```python
import torch

def _softmax(x, axis=-1):
    return torch.softmax(x, dim=axis)
def _gelu(x):
    return torch.nn.functional.gelu(x, approximate="tanh")
def _silu(x):
    return torch.nn.functional.silu(x)
def _sigmoid(x):
    return torch.sigmoid(x)
def _layernorm(x, axis=-1, eps=1e-5):
    m = x.mean(dim=axis, keepdim=True)
    v = x.var(dim=axis, keepdim=True, unbiased=False)
    return (x - m) / torch.sqrt(v + eps)
def _scatter_add(tgt, idx, src, axis=0):
    return tgt.clone().index_add_(axis, idx, src)
def _segment_sum(src, seg, n):
    out = torch.zeros((n,) + src.shape[1:], dtype=src.dtype)
    return out.index_add_(0, seg, src)
def _pad(x, pw):
    flat = tuple(v for pair in reversed(pw) for v in pair)
    return torch.nn.functional.pad(x, flat)

kernel_name = 'interleave_layers__sliding_block_cached__mixer_channel_mlp__decode'

def setup_inputs():

    _w = (lambda a: (lambda t: t.to(torch.bfloat16) if t.is_floating_point() else t)(torch.as_tensor(a)))
    def _A_setup():
        B, Sq, D = 8, 1, 1024
        Hq, Hkv, Sk, Dh = 32, 4, 4096, 128
        assert Hq % Hkv == 0, (Hq, Hkv)
        return {
            "x": _w((lambda *s: torch.randn(*s, dtype=torch.bfloat16) * 0.05)(B, Sq, D)),
            "k_cache": _w((lambda *s: torch.randn(*s, dtype=torch.bfloat16) * 1.6)(B, Hkv, Sk, Dh)),
            "v_cache": _w((lambda *s: torch.randn(*s, dtype=torch.bfloat16) * 1.6)(B, Hkv, Sk, Dh)),
            "wq": _w((lambda *s: torch.randn(*s, dtype=torch.bfloat16) * 1.6)(D, Hq * Dh) * (D ** -0.5)),
            "wo": _w((lambda *s: torch.randn(*s, dtype=torch.bfloat16) * 1.6)(Hq * Dh, D) * ((Hq * Dh) ** -0.5)),
        }
    _A_in = _A_setup()

    def _B_setup():
        B, S, D, F = 8, 1, 1024, 3584
        return {
            "x": _w((lambda *s: torch.randn(*s, dtype=torch.bfloat16) * 0.05)(B, S, D)),
            "w1": _w((lambda *s: torch.randn(*s, dtype=torch.bfloat16) * 3.2)(D, F) * (D ** -0.5)),
            "b1": _w((lambda *s: torch.randn(*s, dtype=torch.bfloat16) * 0.8)(F)),
            "w2": _w((lambda *s: torch.randn(*s, dtype=torch.bfloat16) * 0.4)(F, D) * (F ** -0.5)),
            "b2": _w((lambda *s: torch.randn(*s, dtype=torch.bfloat16) * 0.4)(D)),
        }
    _B_in = _B_setup()

    return {
        "A_x": _A_in["x"],
        "A_k_cache": _A_in["k_cache"],
        "A_v_cache": _A_in["v_cache"],
        "A_wq": _A_in["wq"],
        "A_wo": _A_in["wo"],
        "B_x": _B_in["x"],
        "B_w1": _B_in["w1"],
        "B_b1": _B_in["b1"],
        "B_w2": _B_in["w2"],
        "B_b2": _B_in["b2"],
    }

def reference(A_x, A_k_cache, A_v_cache, A_wq, A_wo, B_x, B_w1, B_b1, B_w2, B_b2):
    def _A_ref(x, k_cache, v_cache, wq, wo):
        B, Sq, D = x.shape
        Hq, Dh = 32, 128
        Hkv, Sk = k_cache.shape[1], k_cache.shape[2]
        W = min(4096, max(256, Sk // 4))
        rep = Hq // Hkv
        q = (lambda x, *d: x.permute(*d))((x @ wq).reshape(B, Sq, Hq, Dh), 0, 2, 1, 3)
        k = (lambda x, n, axis: torch.repeat_interleave(x, n, dim=axis))(k_cache[:, :, Sk - W:, :], rep, axis=1)
        v = (lambda x, n, axis: torch.repeat_interleave(x, n, dim=axis))(v_cache[:, :, Sk - W:, :], rep, axis=1)
        scores = torch.einsum("bhqd,bhkd->bhqk", q, k) * (Dh ** -0.5)
        p = _softmax(scores, axis=-1)
        o = torch.einsum("bhqk,bhkd->bhqd", p, v)
        return (lambda x, *d: x.permute(*d))(o, 0, 2, 1, 3).reshape(B, Sq, Hq * Dh) @ wo

    def _B_ref(x, w1, b1, w2, b2):
        y = _layernorm(x, axis=-1)
        h = _gelu(torch.einsum("bsd,df->bsf", y, w1) + b1)
        return torch.einsum("bsf,fd->bsd", h, w2) + b2

    x = A_x
    x = x + _A_ref(_layernorm(x, axis=-1), A_k_cache, A_v_cache, A_wq, A_wo)
    x = x + _B_ref(_layernorm(x, axis=-1), B_w1, B_b1, B_w2, B_b2)
    x = x + _A_ref(_layernorm(x, axis=-1), A_k_cache, A_v_cache, A_wq, A_wo)
    x = x + _B_ref(_layernorm(x, axis=-1), B_w1, B_b1, B_w2, B_b2)
    return x
```

```cpp
#include <hip/hip_runtime.h>
#include <hip/hip_bf16.h>
#include <stdint.h>
typedef unsigned short u16;
__device__ __forceinline__ float bf2f(u16 b){ return __uint_as_float(((unsigned)b)<<16); }
__device__ __forceinline__ u16 f2bf(float f){ unsigned u=__float_as_uint(f); u += 0x7fff + ((u>>16)&1); return (u16)(u>>16); }
__device__ __forceinline__ float rbf(float f){ return bf2f(f2bf(f)); }

constexpr int B=8, D=1024, HQ=32, HKV=4, SK=4096, DH=128, W=1024, F=3584;

__device__ float block_sum(float v, float* red){
  for(int o=32;o>0;o>>=1) v += __shfl_xor(v,o);
  int w=threadIdx.x>>6, l=threadIdx.x&63;
  __syncthreads();
  if(l==0) red[w]=v;
  __syncthreads();
  float s=0; for(int i=0;i<(int)(blockDim.x>>6);i++) s+=red[i];
  return s;
}
__global__ void k_ln(const float* x, float* y, int times){
  __shared__ float red[16];
  int b=blockIdx.x; float v[4];
  for(int i=0;i<4;i++) v[i]=x[b*D+threadIdx.x+256*i];
  for(int t=0;t<times;t++){
    float s=0; for(int i=0;i<4;i++) s+=v[i];
    float m=block_sum(s,red)/D;
    float q=0; for(int i=0;i<4;i++) q+=(v[i]-m)*(v[i]-m);
    float var=block_sum(q,red)/D;
    float r=1.0f/sqrtf(var+1e-5f);
    for(int i=0;i<4;i++) v[i]=rbf((v[i]-m)*r);
  }
  for(int i=0;i<4;i++) y[b*D+threadIdx.x+256*i]=v[i];
}
__global__ void k_cvt_in(const u16* x, float* y){ int i=blockIdx.x*256+threadIdx.x; y[i]=bf2f(x[i]); }
__global__ void k_cvt_out(const float* x, u16* y){ int i=blockIdx.x*256+threadIdx.x; y[i]=f2bf(x[i]); }
__global__ void k_gemv(const float* x, const u16* w, const u16* bias, const float* res, float* y, int K, int N, int act){
  __shared__ float sm[4][B][64];
  int c=threadIdx.x&63, g=threadIdx.x>>6; int n=blockIdx.x*64+c;
  float acc[B]; for(int b=0;b<B;b++) acc[b]=0;
  for(int k=g;k<K;k+=4){ float wv=bf2f(w[(size_t)k*N+n]); for(int b=0;b<B;b++) acc[b]+=x[b*K+k]*wv; }
  for(int b=0;b<B;b++) sm[g][b][c]=acc[b];
  __syncthreads();
  for(int b=g;b<B;b+=4){
    float s=sm[0][b][c]+sm[1][b][c]+sm[2][b][c]+sm[3][b][c];
    s=rbf(s);
    if(bias) s=rbf(s+bf2f(bias[n]));
    if(act){ float u=0.7978845608028654f*(s+0.044715f*s*s*s); s=rbf(0.5f*s*(1.0f+tanhf(u))); }
    if(res) s=rbf(s+res[b*N+n]);
    y[b*N+n]=s;
  }
}
__global__ void k_attn(const float* q, const u16* kc, const u16* vc, float* o){
  __shared__ float s[W]; __shared__ float qs[DH]; __shared__ float red[16]; __shared__ float part[2][DH];
  int b=blockIdx.x/HQ, h=blockIdx.x%HQ, hk=h/(HQ/HKV);
  const u16* K=kc+((size_t)(b*HKV+hk)*SK+(SK-W))*DH; const u16* V=vc+((size_t)(b*HKV+hk)*SK+(SK-W))*DH;
  if(threadIdx.x<DH) qs[threadIdx.x]=q[b*HQ*DH+h*DH+threadIdx.x];
  __syncthreads();
  float mx=-INFINITY;
  for(int j=threadIdx.x;j<W;j+=256){ float a=0; for(int d=0;d<DH;d++) a+=qs[d]*bf2f(K[(size_t)j*DH+d]); a=rbf(rbf(a)*0.08838834764831845f); s[j]=a; mx=fmaxf(mx,a);}
  for(int o2=32;o2>0;o2>>=1) mx=fmaxf(mx,__shfl_xor(mx,o2));
  __syncthreads(); if((threadIdx.x&63)==0) red[threadIdx.x>>6]=mx; __syncthreads();
  mx=fmaxf(fmaxf(red[0],red[1]),fmaxf(red[2],red[3]));
  float sum=0; for(int j=threadIdx.x;j<W;j+=256){ float e=__expf(s[j]-mx); s[j]=e; sum+=e; }
  sum=block_sum(sum,red);
  float inv=1.0f/sum;
  int d=threadIdx.x&127, g=threadIdx.x>>7; float a=0;
  for(int j=g;j<W;j+=2) a+=rbf(s[j]*inv)*bf2f(V[(size_t)j*DH+d]);
  part[g][d]=a; __syncthreads();
  if(threadIdx.x<DH) o[b*HQ*DH+h*DH+threadIdx.x]=rbf(part[0][d]+part[1][d]);
}
extern "C" void kernel_launch(void* const* d_in, const int* in_sizes, int n_in, void* d_out, int out_size, void* d_ws, size_t ws_size, hipStream_t stream){
  const u16* x=(const u16*)d_in[0]; const u16* kc=(const u16*)d_in[1]; const u16* vc=(const u16*)d_in[2];
  const u16* wq=(const u16*)d_in[3]; const u16* wo=(const u16*)d_in[4];
  const u16* w1=(const u16*)d_in[6]; const u16* b1=(const u16*)d_in[7]; const u16* w2=(const u16*)d_in[8]; const u16* b2=(const u16*)d_in[9];
  float* ws=(float*)d_ws; float* X=ws; float* Y=ws+8192; float* Q=ws+16384; float* O=Q+32768; float* H=O+32768;
  k_cvt_in<<<32,256,0,stream>>>(x,X);
  for(int l=0;l<2;l++){
    k_ln<<<B,256,0,stream>>>(X,Y,1);
    k_gemv<<<HQ*DH/64,256,0,stream>>>(Y,wq,nullptr,nullptr,Q,D,HQ*DH,0);
    k_attn<<<B*HQ,256,0,stream>>>(Q,kc,vc,O);
    k_gemv<<<D/64,256,0,stream>>>(O,wo,nullptr,X,X,HQ*DH,D,0);
    k_ln<<<B,256,0,stream>>>(X,Y,2);
    k_gemv<<<F/64,256,0,stream>>>(Y,w1,b1,nullptr,H,D,F,1);
    k_gemv<<<D/64,256,0,stream>>>(H,w2,b2,X,X,F,D,0);
  }
  k_cvt_out<<<32,256,0,stream>>>(X,(u16*)d_out);
}
```

```cpp
#include <hip/hip_runtime.h>
#include <stdint.h>

namespace {
typedef unsigned short u16;
typedef unsigned v4u __attribute__((ext_vector_type(4)));
typedef unsigned v2u __attribute__((ext_vector_type(2)));
typedef float f4 __attribute__((ext_vector_type(4)));
typedef float f2 __attribute__((ext_vector_type(2)));
typedef short s8v __attribute__((ext_vector_type(8)));
typedef short s4v __attribute__((ext_vector_type(4)));
typedef __bf16 bf2 __attribute__((ext_vector_type(2)));
#define LAS __attribute__((address_space(3)))
#define GAS __attribute__((address_space(1)))
#define DEV __device__ __forceinline__

constexpr int NCU = 256, NT = 512;
constexpr int DM = 1024, SK = 4096, WIN = 1024, DFF = 3584;

constexpr unsigned WS_GEN = 0;
constexpr unsigned WS_Q0 = 4096, Q0S = 4096 + 128;
constexpr unsigned WS_R = WS_Q0 + NCU * Q0S, RS = 28800;
constexpr unsigned R_OB0 = 0, R_LSE0 = 2048, R_X10 = 2176, R_XP0 = 3072;
constexpr unsigned R_HP0 = 7168, R_X2P0 = 9216, R_X2 = 13312, R_LSE1 = 13440, R_X11 = 13568;
constexpr unsigned R_Q1 = 14336, R_OB1 = 16384, R_XP1 = 18432, R_HP1 = 22528, R_X2P1 = 24576;
constexpr unsigned R_BCLR0 = 7168, R_BCLRN = 21;
constexpr unsigned WS_BYTES = WS_R + NCU * RS;
constexpr unsigned WS_STAMP = 16u << 20;
constexpr unsigned SENT = 0xFFFFFFFFu;
constexpr int AUX_SC1 = 16;

constexpr unsigned L_S0 = 0, L_S1 = 32768, L_S2 = 65536, L_S3 = 98304;
constexpr unsigned YS = 544, AS = 288;
constexpr unsigned L_Y = 131072, L_Q = L_Y + 8 * YS, L_P = L_Q + 8 * AS, L_O = L_P + 8 * AS, L_A = L_O + 8 * AS;
constexpr unsigned L_M = L_A + 8 * AS, L_L = L_M + 256, L_END = L_L + 256;
constexpr unsigned LDS_BYTES = 147456;
static_assert(L_END <= LDS_BYTES, "LDS map");

constexpr float EPS = 1e-5f, LOG2E = 1.4426950408889634f, QK_SCALE = 0.08838834764831845f;
constexpr unsigned SPIN_CAP = 1u << 14;

#define CODE_LINES 64
#define D_Q0 4
#define D_Q1 8
#define D_O 8
#define D_XP 8
#define D_X1A 8
#define D_X1B 30
#define D_HP 8
#define D_X2P 8
#define D_X2A 8
#define D_X2B 30

struct Params {
  const u16 *x, *kc, *vc, *wq, *wo, *w1, *b1, *w2, *b2;
  u16 *out;
  unsigned char *ws;
};

DEV float lo16(unsigned v) { return __uint_as_float(v << 16); }
DEV float hi16(unsigned v) { return __uint_as_float(v & 0xffff0000u); }
DEV unsigned pk(float a, float b) { f2 v = {a, b}; return __builtin_bit_cast(unsigned, __builtin_convertvector(v, bf2)); }
DEV float rbf(float a) { return lo16(pk(a, a)); }
DEV unsigned nosent(unsigned v) { return v < 0xFFFFFFFEu ? v : 0xFFFFFFFEu; }
DEV unsigned nosentf(float v) { return nosent(__float_as_uint(v)); }
DEV float dot2(unsigned a, unsigned b, float c) {
  return __builtin_amdgcn_fdot2_f32_bf16(__builtin_bit_cast(bf2, a), __builtin_bit_cast(bf2, b), c, false);
}
DEV f4 mfma(v4u a, v4u b, f4 c) {
  return __builtin_amdgcn_mfma_f32_16x16x32_bf16(__builtin_bit_cast(s8v, a), __builtin_bit_cast(s8v, b), c, 0, 0, 0);
}
template <int CTRL, int RM> DEV float dppz(float v) {
  return __builtin_bit_cast(float, __builtin_amdgcn_update_dpp(0, __builtin_bit_cast(int, v), CTRL, RM, 0xf, true));
}
template <int CTRL> DEV float dppm(float v) {
  return __builtin_bit_cast(float, __builtin_amdgcn_mov_dpp(__builtin_bit_cast(int, v), CTRL, 0xf, 0xf, true));
}
DEV float wave_sum(float v) {
  v += dppz<0x111, 0xf>(v); v += dppz<0x112, 0xf>(v); v += dppz<0x114, 0xf>(v); v += dppz<0x118, 0xf>(v);
  v += dppz<0x142, 0xa>(v); v += dppz<0x143, 0xc>(v);
  return __builtin_bit_cast(float, __builtin_amdgcn_readlane(__builtin_bit_cast(int, v), 63));
}
DEV float xrow_max(float x) {
  auto s = __builtin_amdgcn_permlane16_swap(__float_as_uint(x), __float_as_uint(x), false, false);
  x = fmaxf(__uint_as_float(s[0]), __uint_as_float(s[1]));
  auto t = __builtin_amdgcn_permlane32_swap(__float_as_uint(x), __float_as_uint(x), false, false);
  return fmaxf(__uint_as_float(t[0]), __uint_as_float(t[1]));
}
DEV float xrow_sum(float x) {
  auto s = __builtin_amdgcn_permlane16_swap(__float_as_uint(x), __float_as_uint(x), false, false);
  x = __uint_as_float(s[0]) + __uint_as_float(s[1]);
  auto t = __builtin_amdgcn_permlane32_swap(__float_as_uint(x), __float_as_uint(x), false, false);
  return __uint_as_float(t[0]) + __uint_as_float(t[1]);
}
DEV unsigned umax(unsigned a, unsigned b) { return a > b ? a : b; }
DEV unsigned umax4(v4u v) { return umax(umax(v.x, v.y), umax(v.z, v.w)); }
#define BAR() asm volatile("s_waitcnt lgkmcnt(0)\n\ts_barrier" ::: "memory")
#define VMWAIT(n) asm volatile("s_waitcnt vmcnt(" #n ")" ::: "memory")
#define MEMFENCE() asm volatile("" ::: "memory")
template <int N> DEV void nap() {
  if constexpr (N > 120) { __builtin_amdgcn_s_sleep(120); nap<N - 120>(); }
  else if constexpr (N > 0) __builtin_amdgcn_s_sleep(N);
}

DEV void dma16(const void *sbase, unsigned voff, unsigned ldst) {
  unsigned keep;
  asm volatile("s_mov_b32 %0, m0\n\ts_mov_b32 m0, %3\n\ts_nop 0\n\tglobal_load_lds_dwordx4 %1, %2 nt\n\ts_mov_b32 m0, %0"
               : "=&s"(keep) : "v"(voff), "s"(sbase), "s"(ldst) : "memory");
}
DEV void dma_tile128(const unsigned char *base, unsigned rstride, unsigned slot, int w, int l) {
  const unsigned rr = l >> 4, cp = l & 15;
#pragma unroll
  for (int j = 0; j < 4; ++j) {
    const unsigned i = 4 * w + j;
    const unsigned f = rr | (((i >> 1) & 1) << 2);
    const unsigned ch = (((cp >> 1) ^ f) << 1) | (cp & 1);
    dma16(base + (size_t)(4 * i) * rstride, rr * rstride + ch * 16, slot + i * 1024);
  }
}
DEV void dma_tile64(const unsigned char *base, unsigned rstride, unsigned slot, int w, int l) {
  const unsigned rr = l >> 3, cp = l & 7;
#pragma unroll
  for (int j = 0; j < 4; ++j) {
    const unsigned i = 4 * w + j;
    const unsigned f = ((rr >> 1) & 1) | ((i & 1) << 1);
    const unsigned ch = (((cp >> 1) ^ f) << 1) | (cp & 1);
    dma16(base + (size_t)(8 * i) * rstride, rr * rstride + ch * 16, slot + i * 1024);
  }
}
DEV v2u trread(LAS unsigned char *p) {
  return __builtin_bit_cast(v2u, __builtin_amdgcn_ds_read_tr16_b64_v4i16((LAS s4v *)p));
}
DEV void frags128(LAS unsigned char *slot, int m, int l, v4u (&f)[4]) {
  const unsigned g = l >> 4, q = (l & 15) >> 2, p = l & 3;
  const unsigned sw = q | ((g & 1) << 2);
  LAS unsigned char *a = slot + (8 * g + q) * 256 + ((m ^ sw) * 32) + p * 8;
#pragma unroll
  for (int s = 0; s < 4; ++s) {
    const v2u u0 = trread(a + s * 8192), u1 = trread(a + s * 8192 + 1024);
    f[s] = v4u{u0.x, u0.y, u1.x, u1.y};
  }
}
DEV void frags64(LAS unsigned char *slot, int m, int l, v4u (&f)[8]) {
  const unsigned g = l >> 4, q = (l & 15) >> 2, p = l & 3;
  const unsigned sw = ((q >> 1) & 1) | ((g & 1) << 1);
  LAS unsigned char *a = slot + (8 * g + q) * 128 + ((m ^ sw) * 32) + p * 8;
#pragma unroll
  for (int s = 0; s < 8; ++s) {
    const v2u u0 = trread(a + s * 4096), u1 = trread(a + s * 4096 + 512);
    f[s] = v4u{u0.x, u0.y, u1.x, u1.y};
  }
}
DEV v4u lds128(LAS unsigned char *p) { return *(LAS v4u *)p; }

typedef __amdgpu_buffer_rsrc_t rsrc_t;
DEV v4u ld128(rsrc_t r, unsigned off) { return __builtin_amdgcn_raw_buffer_load_b128(r, off, 0, AUX_SC1); }
DEV v2u ld64(rsrc_t r, unsigned off) { return __builtin_amdgcn_raw_buffer_load_b64(r, off, 0, AUX_SC1); }
DEV unsigned ld32(rsrc_t r, unsigned off) { return __builtin_amdgcn_raw_buffer_load_b32(r, off, 0, AUX_SC1); }
DEV void st128(rsrc_t r, unsigned off, v4u v) { __builtin_amdgcn_raw_buffer_store_b128(v, r, off, 0, AUX_SC1); }
DEV void st64(rsrc_t r, unsigned off, v2u v) { __builtin_amdgcn_raw_buffer_store_b64(v, r, off, 0, AUX_SC1); }
DEV void st32(rsrc_t r, unsigned off, unsigned v) { __builtin_amdgcn_raw_buffer_store_b32(v, r, off, 0, AUX_SC1); }
DEV bool all_ok(bool bad) { return __builtin_amdgcn_ballot_w64(bad) == 0ull; }

#define STAMP(i) do {} while (0)
#define STAMPW(i, wv) do {} while (0)

struct Ctx {
  rsrc_t rs;
  LAS unsigned char *L;
  int c, tid, w, l;
  unsigned reg;
  unsigned cap;
};
DEV unsigned region(unsigned c) { return WS_R + c * RS; }

DEV void row_stats(const v2u (&x)[4], float &mean, float &var) {
  float s1 = 0.f, s2 = 0.f;
#pragma unroll
  for (int i = 0; i < 4; ++i) {
    s1 = dot2(x[i].x, 0x3F803F80u, s1); s2 = dot2(x[i].x, x[i].x, s2);
    s1 = dot2(x[i].y, 0x3F803F80u, s1); s2 = dot2(x[i].y, x[i].y, s2);
  }
  s1 = wave_sum(s1); s2 = wave_sum(s2);
  mean = s1 * (1.0f / DM);
  var = fmaxf(s2 * (1.0f / DM) - mean * mean, 0.f);
}
DEV void gather_row(Ctx &C, unsigned roff, int kq, v2u (&x)[4]) {
  unsigned off[4];
#pragma unroll
  for (int i = 0; i < 4; ++i) off[i] = region(C.w + 8 * (4 * ((kq + i) & 3) + (C.l >> 4))) + roff + (C.l & 15) * 8;
  for (unsigned spin = 0;; ++spin) {
    MEMFENCE();
    unsigned m = 0;
#pragma unroll
    for (int i = 0; i < 4; ++i) x[i] = ld64(C.rs, off[i]);
#pragma unroll
    for (int i = 0; i < 4; ++i) m = umax(m, umax(x[i].x, x[i].y));
    if (all_ok(m == SENT)) break;
    if (spin >= C.cap) { C.cap = 0; break; }
  }
  VMWAIT(0);
}

DEV void stage_q_ln(Ctx &C, const v2u (&x)[4]) {
  float mean, var;
  row_stats(x, mean, var);
  const float mb = rbf(mean), sd = rbf(__builtin_amdgcn_sqrtf(rbf(rbf(var) + EPS))), rs = __builtin_amdgcn_rcpf(sd);
  const unsigned d01 = pk(lo16(x[0].x) - mb, hi16(x[0].x) - mb), d23 = pk(lo16(x[0].y) - mb, hi16(x[0].y) - mb);
  v2u y = {pk(lo16(d01) * rs, hi16(d01) * rs), pk(lo16(d23) * rs, hi16(d23) * rs)};
  *(LAS v2u *)(C.L + L_Y + C.w * YS + C.l * 8) = y;
}
template <int LAYER>
DEV void stage_q_mm(Ctx &C, const v4u (&wf)[8], unsigned taglo, unsigned taghi) {
  const int l = C.l, w = C.w;
  if (w < 4) {
    f4 acc = {0.f, 0.f, 0.f, 0.f};
    LAS unsigned char *yb = C.L + L_Y + (l & 7) * YS + (l >> 4) * 16;
#pragma unroll
    for (int s = 0; s < 8; ++s) acc = mfma(wf[s], lds128(yb + s * 64), acc);
    if constexpr (LAYER == 0) {
      const bool up = (l & 8) != 0;
      const float a2 = dppm<0x128>(acc.z), a3 = dppm<0x128>(acc.w);
      const float v0 = up ? a2 : acc.x, v1 = up ? a3 : acc.y;
      const v4u o = {taglo, (__float_as_uint(v0) & 0xFFFFFF00u) | taghi, taglo, (__float_as_uint(v1) & 0xFFFFFF00u) | taghi};
      st128(C.rs, WS_Q0 + C.c * Q0S + w * 1024 + (l & 7) * 128 + ((l >> 3) & 1) * 64 + (l >> 4) * 16, o);
    } else {
      if ((l & 15) < 8) {
        const v4u o = {nosentf(acc.x), nosentf(acc.y), nosentf(acc.z), nosentf(acc.w)};
        st128(C.rs, C.reg + R_Q1 + w * 512 + (l & 15) * 64 + (l >> 4) * 16, o);
      }
    }
  }
}

struct Frags { v4u wqw1[8]; v4u k[4], v[4], wo[4], w2[4]; };

template <int LAYER>
DEV void layer(Ctx &C, const Params &p, Frags &F, unsigned b1v, v2u b2v, v2u &xres, unsigned taglo, unsigned taghi
               , unsigned genlo, unsigned genhi) {
  const int c = C.c, w = C.w, l = C.l, tid = C.tid, g = l >> 4;
  LAS unsigned char *L = C.L;
  const int xc = c & 7, r = c >> 3;
  constexpr unsigned R_OB = LAYER ? R_OB1 : R_OB0, R_LSE = LAYER ? R_LSE1 : R_LSE0, R_XP = LAYER ? R_XP1 : R_XP0;
  constexpr unsigned R_X1 = LAYER ? R_X11 : R_X10, R_HP = LAYER ? R_HP1 : R_HP0, R_X2P = LAYER ? R_X2P1 : R_X2P0;

  {
    const int h = 8 * (r >> 3) + w, ct = 2 * h + (l >> 5), wb = (l >> 3) & 3;
    float q0, q1;
    if constexpr (LAYER == 0) {
      nap<D_Q0>();
      const unsigned off = WS_Q0 + ct * Q0S + wb * 1024 + xc * 128 + (l & 1) * 64 + ((l & 7) >> 1) * 16;
      const unsigned offc = WS_Q0 + (32 * w + (l & 31)) * Q0S;
      v4u v[4];
      for (unsigned spin = 0;; ++spin) {
        MEMFENCE();
#pragma unroll
        for (int i = 0; i < 4; ++i) v[i] = ld128(C.rs, off + i * 64 * Q0S);
        const v2u chk = ld64(C.rs, offc);
        bool bad = chk.x != taglo || (chk.y & 0xFF) != taghi;
#pragma unroll
        for (int i = 0; i < 4; ++i) bad |= v[i].x != taglo || v[i].z != taglo || (v[i].y & 0xFF) != taghi || (v[i].w & 0xFF) != taghi;
        if (all_ok(bad)) break;
        if (spin >= C.cap) { C.cap = 0; break; }
      }
      VMWAIT(0);
      q0 = q1 = 0.f;
#pragma unroll
      for (int i = 0; i < 4; ++i) { q0 += __uint_as_float(v[i].y & 0xFFFFFF00u); q1 += __uint_as_float(v[i].w & 0xFFFFFF00u); }
      asm volatile("" : "+v"(F.k[0]), "+v"(F.k[1]), "+v"(F.k[2]), "+v"(F.k[3]));
    } else {
      nap<D_Q1>();
      const unsigned off = region(ct) + R_Q1 + wb * 512 + xc * 64 + (l & 7) * 8;
      v2u v[4];
      for (unsigned spin = 0;; ++spin) {
        MEMFENCE();
        unsigned m = 0;
#pragma unroll
        for (int i = 0; i < 4; ++i) v[i] = ld64(C.rs, off + i * 64 * RS);
#pragma unroll
        for (int i = 0; i < 4; ++i) m = umax(m, umax(v[i].x, v[i].y));
        if (all_ok(m == SENT)) break;
        if (spin >= C.cap) { C.cap = 0; break; }
      }
      q0 = q1 = 0.f;
#pragma unroll
      for (int i = 0; i < 4; ++i) { q0 += __uint_as_float(v[i].x); q1 += __uint_as_float(v[i].y); }
    }
    *(LAS unsigned *)(L + L_Q + w * AS + l * 4) = pk(q0, q1);
  }
  STAMP(1);
  BAR();
  if constexpr (LAYER == 0) {
    if (c == 0 && tid == 0) st64(C.rs, WS_GEN, v2u{genlo, genhi});
    frags128(L + L_S1, w, l, F.v);
    dma_tile128((const unsigned char *)p.wo + ((size_t)(128 * (xc + 8 * (r >> 3))) * DM + 128 * (r & 7)) * 2, DM * 2, L_S2, w, l);
  }
  {
    const int hh = l & 7;
    const bool low = (l & 15) < 8;
    f4 acc = {0.f, 0.f, 0.f, 0.f};
    LAS unsigned char *qb = L + L_Q + hh * AS + g * 16;
#pragma unroll
    for (int s = 0; s < 4; ++s) acc = mfma(F.k[s], lds128(qb + s * 64), acc);
    float sc[4];
#pragma unroll
    for (int i = 0; i < 4; ++i) sc[i] = rbf(rbf(acc[i]) * QK_SCALE);
    const float wm = xrow_max(fmaxf(fmaxf(sc[0], sc[1]), fmaxf(sc[2], sc[3])));
    if (l < 8) *(LAS float *)(L + L_M + l * 32 + w * 4) = wm;
    BAR();
    const f4 m0 = *(LAS f4 *)(L + L_M + hh * 32), m1 = *(LAS f4 *)(L + L_M + hh * 32 + 16);
    const float M = fmaxf(fmaxf(fmaxf(m0.x, m0.y), fmaxf(m0.z, m0.w)), fmaxf(fmaxf(m1.x, m1.y), fmaxf(m1.z, m1.w)));
    float e[4];
#pragma unroll
    for (int i = 0; i < 4; ++i) e[i] = __builtin_amdgcn_exp2f((sc[i] - M) * LOG2E);
    const float ls = xrow_sum((e[0] + e[1]) + (e[2] + e[3]));
    if (l < 8) *(LAS float *)(L + L_L + l * 32 + w * 4) = ls;
    if (low) *(LAS v2u *)(L + L_P + hh * AS + (16 * w + 4 * g) * 2) = v2u{pk(e[0], e[1]), pk(e[2], e[3])};
    BAR();
    LAS unsigned char *pb = L + L_P + hh * AS + g * 16;
    f4 o = {0.f, 0.f, 0.f, 0.f};
#pragma unroll
    for (int s = 0; s < 4; ++s) o = mfma(F.v[s], lds128(pb + s * 64), o);
    const f4 l0 = *(LAS f4 *)(L + L_L + hh * 32), l1 = *(LAS f4 *)(L + L_L + hh * 32 + 16);
    const float Ls = ((l0.x + l0.y) + (l0.z + l0.w)) + ((l1.x + l1.y) + (l1.z + l1.w));
    const float inv = __builtin_amdgcn_rcpf(Ls);
    if (low) *(LAS v2u *)(L + L_O + hh * AS + (16 * w + 4 * g) * 2) = v2u{pk(o.x * inv, o.y * inv), pk(o.z * inv, o.w * inv)};
    if (w == 2 && l < 8) st32(C.rs, C.reg + R_LSE + l * 4, nosentf(M * LOG2E + __builtin_amdgcn_logf(Ls)));
    BAR();
    if (w < 2) {
      const v4u v = lds128(L + L_O + (tid >> 4) * AS + (tid & 15) * 16);
      st128(C.rs, C.reg + R_OB + tid * 16, v4u{nosent(v.x), nosent(v.y), nosent(v.z), nosent(v.w)});
    }
  }
  STAMP(2);
  if constexpr (LAYER == 0) {
#pragma unroll
    for (int i = 0; i < 3; ++i) {
      const int j = w + 8 * i;
      if (j < (int)R_BCLRN) st128(C.rs, C.reg + R_BCLR0 + j * 1024 + l * 16, v4u{SENT, SENT, SENT, SENT});
    }
  }
  {
    nap<D_O>();
    const int h = xc + 8 * (r >> 3);
    const unsigned base = region(w + 64 * (h >> 3));
    const unsigned off = base + R_OB + (h & 7) * 256 + l * 4;
    const unsigned offl = base + (l & 7) * 8 * RS + R_LSE + (h & 7) * 4;
    unsigned v[8], lse;
    for (unsigned spin = 0;; ++spin) {
      MEMFENCE();
      lse = ld32(C.rs, offl);
#pragma unroll
      for (int s = 0; s < 8; ++s) v[s] = ld32(C.rs, off + s * 8 * RS);
      unsigned m = lse;
#pragma unroll
      for (int s = 0; s < 8; ++s) m = umax(m, v[s]);
      if (all_ok(m == SENT)) break;
      if (spin >= C.cap) { C.cap = 0; break; }
    }
    VMWAIT(0);
    const float ls = __uint_as_float(lse);
    float mx = fmaxf(ls, dppm<0xB1>(ls)); mx = fmaxf(mx, dppm<0x4E>(mx)); mx = fmaxf(mx, dppm<0x141>(mx));
    const float e = __builtin_amdgcn_exp2f(ls - mx);
    float sm = e + dppm<0xB1>(e); sm += dppm<0x4E>(sm); sm += dppm<0x141>(sm);
    const float wt = e * __builtin_amdgcn_rcpf(sm);
    float o0 = 0.f, o1 = 0.f;
#pragma unroll
    for (int s = 0; s < 8; ++s) {
      const float ws = __builtin_bit_cast(float, __builtin_amdgcn_readlane(__builtin_bit_cast(int, wt), s));
      o0 = fmaf(ws, lo16(v[s]), o0); o1 = fmaf(ws, hi16(v[s]), o1);
    }
    *(LAS unsigned *)(L + L_A + w * AS + l * 4) = pk(o0, o1);
    STAMP(3);
    BAR();
    if constexpr (LAYER == 0) {
      frags128(L + L_S2, w, l, F.wo);
      const int c4 = c < 224 ? c : 0;
      dma_tile64((const unsigned char *)p.w1 + ((size_t)(256 * (c4 / 56)) * DFF + 64 * (c4 % 56)) * 2, DFF * 2, L_S3, w, l);
    }
    f4 acc = {0.f, 0.f, 0.f, 0.f};
    LAS unsigned char *ab = L + L_A + (l & 7) * AS + g * 16;
#pragma unroll
    for (int s = 0; s < 4; ++s) acc = mfma(F.wo[s], lds128(ab + s * 64), acc);
    if ((l & 15) < 8)
      st128(C.rs, C.reg + R_XP + w * 512 + (l & 15) * 64 + g * 16, v4u{nosentf(acc.x), nosentf(acc.y), nosentf(acc.z), nosentf(acc.w)});
  }
  STAMP(4);
  const int t3 = c & 7, cc = (c >> 3) & 15;
  const bool reducer = c < 128 && w == 7;
  if (reducer) {
    nap<D_XP>();
    const int hg = l >> 4, j = cc >> 1;
    v4u v[8];
    unsigned off[2];
    off[0] = region(hg + 8 * j) + R_XP + (4 * (cc & 1) + ((l >> 2) & 3)) * 512 + t3 * 64 + (l & 3) * 16;
    off[1] = off[0] + 4 * RS;
    for (unsigned spin = 0;; ++spin) {
      MEMFENCE();
      unsigned m = 0;
#pragma unroll
      for (int i = 0; i < 8; ++i) v[i] = ld128(C.rs, off[i & 1] + (i >> 1) * 64 * RS);
#pragma unroll
      for (int i = 0; i < 8; ++i) m = umax(m, umax4(v[i]));
      if (all_ok(m == SENT)) break;
      if (spin >= C.cap) { C.cap = 0; break; }
    }
    f4 a = {0.f, 0.f, 0.f, 0.f};
#pragma unroll
    for (int i = 0; i < 8; ++i) a += __builtin_bit_cast(f4, v[i]);
    const float a0 = rbf(xrow_sum(a.x)), a1 = rbf(xrow_sum(a.y)), a2 = rbf(xrow_sum(a.z)), a3 = rbf(xrow_sum(a.w));
    xres = v2u{pk(lo16(xres.x) + a0, hi16(xres.x) + a1), pk(lo16(xres.y) + a2, hi16(xres.y) + a3)};
    if (l < 16) st64(C.rs, C.reg + R_X1 + l * 8, v2u{nosent(xres.x), nosent(xres.y)});
    STAMPW(5, 7);
    nap<D_X1A>();
  } else {
    nap<D_X1B>();
  }
  {
    const bool act = c < 224;
    const int kq = act ? c / 56 : 0;
    v2u x[4];
    gather_row(C, R_X1, kq, x);
    STAMP(6);
    float mean, var;
    row_stats(x, mean, var);
    const float r1 = __builtin_amdgcn_rsqf(var + EPS), v2 = var * r1 * r1, rr = r1 * __builtin_amdgcn_rsqf(v2 + EPS);
    const v2u y = {pk((lo16(x[0].x) - mean) * rr, (hi16(x[0].x) - mean) * rr), pk((lo16(x[0].y) - mean) * rr, (hi16(x[0].y) - mean) * rr)};
    *(LAS v2u *)(L + L_Y + w * YS + l * 8) = y;
    BAR();
    if constexpr (LAYER == 0) {
      if (w >= 4) frags64(L + L_S3, w - 4, l, F.wqw1);
      const int kr = xc + 8 * (r >> 3), kra = kr < 28 ? kr : 0;
      dma_tile128((const unsigned char *)p.w2 + ((size_t)(128 * kra) * DM + 128 * (r & 7)) * 2, DM * 2, L_S0, w, l);
    }
    if (w >= 4 && act) {
      f4 acc = {0.f, 0.f, 0.f, 0.f};
      LAS unsigned char *yb = L + L_Y + (l & 7) * YS + g * 16;
#pragma unroll
      for (int s = 0; s < 8; ++s) acc = mfma(F.wqw1[s], lds128(yb + s * 64), acc);
      if ((l & 15) < 8)
        st128(C.rs, C.reg + R_HP + (w - 4) * 512 + (l & 15) * 64 + g * 16, v4u{nosentf(acc.x), nosentf(acc.y), nosentf(acc.z), nosentf(acc.w)});
    }
  }
  STAMP(7);
  {
    const int kr = xc + 8 * (r >> 3);
    const bool act = kr < 28;
    if (act) {
      nap<D_HP>();
      const unsigned off = region(2 * kr + (w >> 2)) + R_HP + (w & 3) * 512 + l * 8;
      v2u v[4];
      for (unsigned spin = 0;; ++spin) {
        MEMFENCE();
        unsigned m = 0;
#pragma unroll
        for (int i = 0; i < 4; ++i) v[i] = ld64(C.rs, off + i * 56 * RS);
#pragma unroll
        for (int i = 0; i < 4; ++i) m = umax(m, umax(v[i].x, v[i].y));
        if (all_ok(m == SENT)) break;
        if (spin >= C.cap) { C.cap = 0; break; }
      }
      float h0 = 0.f, h1 = 0.f;
#pragma unroll
      for (int i = 0; i < 4; ++i) { h0 += __uint_as_float(v[i].x); h1 += __uint_as_float(v[i].y); }
      h0 = rbf(rbf(h0) + lo16(b1v)); h1 = rbf(rbf(h1) + hi16(b1v));
      const float u0 = h0 * (1.0f + 0.044715f * h0 * h0), u1 = h1 * (1.0f + 0.044715f * h1 * h1);
      constexpr float GC = -2.0f * 0.7978845608028654f * LOG2E;
      const float g0 = h0 * __builtin_amdgcn_rcpf(1.0f + __builtin_amdgcn_exp2f(GC * u0));
      const float g1 = h1 * __builtin_amdgcn_rcpf(1.0f + __builtin_amdgcn_exp2f(GC * u1));
      *(LAS unsigned *)(L + L_A + (l >> 3) * AS + (16 * w + 2 * (l & 7)) * 2) = pk(g0, g1);
    }
    VMWAIT(0);
    STAMP(8);
    BAR();
    if constexpr (LAYER == 0) frags128(L + L_S0, w, l, F.w2);
    if (act) {
      f4 acc = {0.f, 0.f, 0.f, 0.f};
      LAS unsigned char *ab = L + L_A + (l & 7) * AS + g * 16;
#pragma unroll
      for (int s = 0; s < 4; ++s) acc = mfma(F.w2[s], lds128(ab + s * 64), acc);
      if ((l & 15) < 8)
        st128(C.rs, C.reg + R_X2P + w * 512 + (l & 15) * 64 + g * 16, v4u{nosentf(acc.x), nosentf(acc.y), nosentf(acc.z), nosentf(acc.w)});
    }
  }
  STAMP(9);
  if (reducer) {
    nap<D_X2P>();
    const int hg = l >> 4, j = cc >> 1;
    v4u v[7];
    unsigned off[2];
    off[0] = region(hg + 8 * j) + R_X2P + (4 * (cc & 1) + ((l >> 2) & 3)) * 512 + t3 * 64 + (l & 3) * 16;
    off[1] = off[0] + 4 * RS;
    for (unsigned spin = 0;; ++spin) {
      MEMFENCE();
      unsigned m = 0;
#pragma unroll
      for (int i = 0; i < 7; ++i) v[i] = ld128(C.rs, off[i & 1] + (i >> 1) * 64 * RS);
#pragma unroll
      for (int i = 0; i < 7; ++i) m = umax(m, umax4(v[i]));
      if (all_ok(m == SENT)) break;
      if (spin >= C.cap) { C.cap = 0; break; }
    }
    f4 a = {0.f, 0.f, 0.f, 0.f};
#pragma unroll
    for (int i = 0; i < 7; ++i) a += __builtin_bit_cast(f4, v[i]);
    const float a0 = rbf(rbf(xrow_sum(a.x)) + lo16(b2v.x)), a1 = rbf(rbf(xrow_sum(a.y)) + hi16(b2v.x));
    const float a2 = rbf(rbf(xrow_sum(a.z)) + lo16(b2v.y)), a3 = rbf(rbf(xrow_sum(a.w)) + hi16(b2v.y));
    xres = v2u{pk(lo16(xres.x) + a0, hi16(xres.x) + a1), pk(lo16(xres.y) + a2, hi16(xres.y) + a3)};
    if constexpr (LAYER == 0) {
      if (l < 16) st64(C.rs, C.reg + R_X2 + l * 8, v2u{nosent(xres.x), nosent(xres.y)});
    } else {
      if (l < 16) *(GAS v2u *)(p.out + t3 * DM + 64 * cc + 4 * l) = xres;
    }
    STAMPW(10, 7);
    if constexpr (LAYER == 0) nap<D_X2A>();
  } else {
    if constexpr (LAYER == 0) nap<D_X2B>();
  }
  if constexpr (LAYER == 0) {
    v2u x[4];
    gather_row(C, R_X2, c >> 6, x);
    STAMP(11);
    stage_q_ln(C, x);
    BAR();
    stage_q_mm<1>(C, F.wqw1, 0, 0);
    STAMP(12);
  }
}

__global__ void __launch_bounds__(NT, 2) fused_decode_kernel(Params p) {
  __shared__ __attribute__((aligned(16))) unsigned char smem[LDS_BYTES];
  Ctx C;
  C.L = (LAS unsigned char *)smem;
  C.c = blockIdx.x; C.tid = threadIdx.x; C.l = threadIdx.x & 63;
  C.w = __builtin_amdgcn_readfirstlane(threadIdx.x >> 6);
  C.rs = __builtin_amdgcn_make_buffer_rsrc((void *)p.ws, 0, WS_BYTES, 0x00020000);
  C.reg = region(C.c);
  C.cap = SPIN_CAP;
  const int c = C.c, w = C.w, l = C.l, tid = C.tid, xc = c & 7, r = c >> 3;
  STAMP(0);
  unsigned warm;
  {
    unsigned long long pc;
    asm volatile("s_getpc_b64 %0" : "=s"(pc));
    unsigned line = r * 8 + w;
    line = line < CODE_LINES ? line : CODE_LINES - 1;
    const unsigned voff = line * 128;
    asm volatile("global_load_dword %0, %1, %2" : "=v"(warm) : "v"(voff), "s"(pc) : "memory");
  }
  v2u gen, x[4];
  {
    const unsigned z = 0;
    asm volatile("global_load_dwordx2 %0, %1, %2 sc1" : "=v"(gen) : "v"(z), "s"(p.ws) : "memory");
    const int kq = c >> 6;
#pragma unroll
    for (int i = 0; i < 4; ++i) {
      const unsigned voff = w * 2048 + ((kq + i) & 3) * 512 + l * 8;
      asm volatile("global_load_dwordx2 %0, %1, %2" : "=v"(x[i]) : "v"(voff), "s"(p.x) : "memory");
    }
  }
  st128(C.rs, C.reg + w * 1024 + l * 16, v4u{SENT, SENT, SENT, SENT});
  MEMFENCE();
  dma_tile64((const unsigned char *)p.wq + ((size_t)(256 * (c >> 6)) * 4096 + 64 * (c & 63)) * 2, 8192, L_S0, w, l);
  Frags F;
  const size_t kvrow = (size_t)(xc * 4 + (r >> 3)) * SK + (SK - WIN) + 128 * (r & 7);
  {
    const unsigned char *kb = (const unsigned char *)p.kc + (kvrow + 16 * w) * 256;
    const unsigned voff = (l & 15) * 256 + (l >> 4) * 16;
    asm volatile("global_load_dwordx4 %0, %1, %2 nt" : "=v"(F.k[0]) : "v"(voff), "s"(kb) : "memory");
    asm volatile("global_load_dwordx4 %0, %1, %2 offset:64 nt" : "=v"(F.k[1]) : "v"(voff), "s"(kb) : "memory");
    asm volatile("global_load_dwordx4 %0, %1, %2 offset:128 nt" : "=v"(F.k[2]) : "v"(voff), "s"(kb) : "memory");
    asm volatile("global_load_dwordx4 %0, %1, %2 offset:192 nt" : "=v"(F.k[3]) : "v"(voff), "s"(kb) : "memory");
  }
  dma_tile128((const unsigned char *)p.vc + kvrow * 256, 256, L_S1, w, l);
  const int kr = xc + 8 * (r >> 3), kra = kr < 28 ? kr : 0, cc = (c >> 3) & 15;
  const unsigned b1v = *(const GAS unsigned *)(p.b1 + 128 * kra + 16 * w + 2 * (l & 7));
  const v2u b2v = *(const GAS v2u *)(p.b2 + 64 * cc + 4 * (l & 15));
  v2u xres = *(const GAS v2u *)(p.x + (c & 7) * DM + 64 * cc + 4 * (l & 15));
  MEMFENCE();
  asm volatile("s_waitcnt vmcnt(16)" : "+v"(gen), "+v"(x[0]), "+v"(x[1]), "+v"(x[2]), "+v"(x[3]), "+v"(warm) :: "memory");
  const unsigned glo = __builtin_amdgcn_readfirstlane(gen.x), ghi = __builtin_amdgcn_readfirstlane(gen.y);
  const unsigned genlo = glo + 1, genhi = ghi + (genlo == 0 ? 1 : 0);
  const unsigned taglo = genlo, taghi = genhi & 0xFF;
#pragma unroll
  for (int s = 0; s < 8; ++s) asm volatile("" : "=v"(F.wqw1[s]));
  STAMP(1);
  stage_q_ln(C, x);
  VMWAIT(11);
  BAR();
  STAMP(2);
  if (w < 4) frags64(C.L + L_S0, w, l, F.wqw1);
  stage_q_mm<0>(C, F.wqw1, taglo, taghi);
  STAMP(3);
  layer<0>(C, p, F, b1v, b2v, xres, taglo, taghi, genlo, genhi);
  layer<1>(C, p, F, b1v, b2v, xres, taglo, taghi, genlo, genhi);
}

}

extern "C" void kernel_launch(void *const *d_in, const int *in_sizes, int n_in, void *d_out, int out_size, void *d_ws,
                              size_t ws_size, hipStream_t stream) {
  Params p;
  p.x = (const u16 *)d_in[0]; p.kc = (const u16 *)d_in[1]; p.vc = (const u16 *)d_in[2];
  p.wq = (const u16 *)d_in[3]; p.wo = (const u16 *)d_in[4];
  p.w1 = (const u16 *)d_in[6]; p.b1 = (const u16 *)d_in[7]; p.w2 = (const u16 *)d_in[8]; p.b2 = (const u16 *)d_in[9];
  p.out = (u16 *)d_out; p.ws = (unsigned char *)d_ws;
  hipLaunchKernelGGL(fused_decode_kernel, dim3(NCU), dim3(NT), 0, stream, p);
}
```

```cpp
#include <hip/hip_runtime.h>
#include <stdint.h>

namespace {
typedef unsigned short u16;
typedef unsigned v4u __attribute__((ext_vector_type(4)));
typedef unsigned v2u __attribute__((ext_vector_type(2)));
typedef float f4 __attribute__((ext_vector_type(4)));
typedef float f2 __attribute__((ext_vector_type(2)));
typedef short s8v __attribute__((ext_vector_type(8)));
typedef short s4v __attribute__((ext_vector_type(4)));
typedef __bf16 bf2 __attribute__((ext_vector_type(2)));
typedef __amdgpu_buffer_rsrc_t rsrc_t;
#define LAS __attribute__((address_space(3)))
#define GAS __attribute__((address_space(1)))
#define DEV __device__ __forceinline__

constexpr int NCU = 256, NT = 512;
constexpr int DM = 1024, SK = 4096, WIN = 1024, DFF = 3584;

constexpr unsigned WS_GEN = 0;
constexpr unsigned WS_Q0 = 4096, Q0S = 4096 + 128;
constexpr unsigned WS_R = WS_Q0 + NCU * Q0S, RS = 28800;
constexpr unsigned R_OB0 = 0, R_LSE0 = 2048, R_X10 = 2176, R_XP0 = 3072;
constexpr unsigned R_HP0 = 7168, R_X2P0 = 9216, R_X2 = 13312, R_LSE1 = 13440, R_X11 = 13568;
constexpr unsigned R_Q1 = 14336, R_OB1 = 16384, R_XP1 = 18432, R_HP1 = 22528, R_X2P1 = 24576;
constexpr unsigned R_BCLR0 = 7168, R_BCLRN = 21;
constexpr unsigned WS_BYTES = WS_R + NCU * RS;
constexpr unsigned WS_STAMP = 16u << 20;
constexpr unsigned SENT = 0xFFFFFFFFu;
constexpr int AUX_SC1 = 16;

constexpr unsigned L_S0 = 0, L_S1 = 32768, L_S2 = 65536, L_S3 = 98304;
constexpr unsigned YS = 544, AS = 288;
constexpr unsigned L_Y = 131072, L_Q = L_Y + 8 * YS, L_P = L_Q + 8 * AS, L_O = L_P + 8 * AS, L_A = L_O + 8 * AS;
constexpr unsigned L_M = L_A + 8 * AS, L_L = L_M + 256, L_END = L_L + 256;
constexpr unsigned LDS_BYTES = 147456;
static_assert(L_END + 512 <= LDS_BYTES, "LDS map");

constexpr float EPS = 1e-5f, LOG2E = 1.4426950408889634f, QK_SCALE = 0.08838834764831845f;
constexpr unsigned SPIN_CAP = 1u << 14;

#define CODE_LINES 150
enum { D_Q0, D_O0, D_XP0, D_X1A0, D_X1B0, D_HP0, D_X2P0, D_X2A0, D_X2B0, D_Q1, D_O1, D_XP1, D_X1A1, D_X1B1, D_HP1, D_X2P1, D_N };
#define DELAYS 4, 8, 8, 8, 30, 8, 8, 8, 30, 8, 8, 8, 8, 30, 8, 8

struct Params {
  const u16 *x, *kc, *vc, *wq, *wo, *w1, *b1, *w2, *b2;
  u16 *out;
  unsigned char *ws;
};

DEV float lo16(unsigned v) { return __uint_as_float(v << 16); }
DEV float hi16(unsigned v) { return __uint_as_float(v & 0xffff0000u); }
DEV unsigned pk(float a, float b) { f2 v = {a, b}; return __builtin_bit_cast(unsigned, __builtin_convertvector(v, bf2)); }
DEV float rbf(float a) { return lo16(pk(a, a)); }
DEV unsigned nosent(unsigned v) { return v < 0xFFFFFFFEu ? v : 0xFFFFFFFEu; }
DEV unsigned nosentf(float v) { return nosent(__float_as_uint(v)); }
DEV v4u nosent4(f4 a) { return v4u{nosentf(a.x), nosentf(a.y), nosentf(a.z), nosentf(a.w)}; }
DEV float dot2(unsigned a, unsigned b, float c) {
  return __builtin_amdgcn_fdot2_f32_bf16(__builtin_bit_cast(bf2, a), __builtin_bit_cast(bf2, b), c, false);
}
DEV f4 mfma(v4u a, v4u b, f4 c) {
  return __builtin_amdgcn_mfma_f32_16x16x32_bf16(__builtin_bit_cast(s8v, a), __builtin_bit_cast(s8v, b), c, 0, 0, 0);
}
DEV float vmax(float a, float b) { float r; asm("v_max_f32 %0, %1, %2" : "=v"(r) : "v"(a), "v"(b)); return r; }
DEV float vmax3(float a, float b, float c) { float r; asm("v_max3_f32 %0, %1, %2, %3" : "=v"(r) : "v"(a), "v"(b), "v"(c)); return r; }
template <int CTRL, int RM> DEV float dppz(float v) {
  return __builtin_bit_cast(float, __builtin_amdgcn_update_dpp(0, __builtin_bit_cast(int, v), CTRL, RM, 0xf, true));
}
template <int CTRL> DEV float dppm(float v) {
  return __builtin_bit_cast(float, __builtin_amdgcn_mov_dpp(__builtin_bit_cast(int, v), CTRL, 0xf, 0xf, true));
}
DEV float rdlane(float v, int i) { return __builtin_bit_cast(float, __builtin_amdgcn_readlane(__builtin_bit_cast(int, v), i)); }
DEV void wave_sum2(float &a, float &b) {
  a += dppz<0x111, 0xf>(a); b += dppz<0x111, 0xf>(b); a += dppz<0x112, 0xf>(a); b += dppz<0x112, 0xf>(b);
  a += dppz<0x114, 0xf>(a); b += dppz<0x114, 0xf>(b); a += dppz<0x118, 0xf>(a); b += dppz<0x118, 0xf>(b);
  a += dppz<0x142, 0xa>(a); b += dppz<0x142, 0xa>(b); a += dppz<0x143, 0xc>(a); b += dppz<0x143, 0xc>(b);
  a = rdlane(a, 63); b = rdlane(b, 63);
}
DEV float xrow_max(float x) {
  auto s = __builtin_amdgcn_permlane16_swap(__float_as_uint(x), __float_as_uint(x), false, false);
  x = vmax(__uint_as_float(s[0]), __uint_as_float(s[1]));
  auto t = __builtin_amdgcn_permlane32_swap(__float_as_uint(x), __float_as_uint(x), false, false);
  return vmax(__uint_as_float(t[0]), __uint_as_float(t[1]));
}
DEV float xrow_sum(float x) {
  auto s = __builtin_amdgcn_permlane16_swap(__float_as_uint(x), __float_as_uint(x), false, false);
  x = __uint_as_float(s[0]) + __uint_as_float(s[1]);
  auto t = __builtin_amdgcn_permlane32_swap(__float_as_uint(x), __float_as_uint(x), false, false);
  return __uint_as_float(t[0]) + __uint_as_float(t[1]);
}
DEV unsigned umax(unsigned a, unsigned b) { return a > b ? a : b; }
DEV unsigned umax4(v4u v) { return umax(umax(v.x, v.y), umax(v.z, v.w)); }
#define BAR() asm volatile("s_waitcnt lgkmcnt(0)\n\ts_barrier" ::: "memory")
#define VMWAIT(n) asm volatile("s_waitcnt vmcnt(" #n ")" ::: "memory")
#define MEMFENCE() asm volatile("" ::: "memory")
#define SCHED() __builtin_amdgcn_sched_barrier(0)
template <int N> DEV void nap_c() {
  if constexpr (N > 120) { __builtin_amdgcn_s_sleep(120); nap_c<N - 120>(); }
  else if constexpr (N > 0) __builtin_amdgcn_s_sleep(N);
}
constexpr int kDelays[D_N] = {DELAYS};
#define NAP(i) nap_c<kDelays[i]>()

DEV void dma16(rsrc_t r, unsigned voff, unsigned soff, unsigned ldst) {
  asm volatile("s_mov_b32 m0, %0\n\ts_nop 3\n\tbuffer_load_dwordx4 %1, %2, %3 offen nt lds"
               :: "s"(ldst), "v"(voff), "s"(r), "s"(soff) : "memory", "m0");
}
DEV rsrc_t mkrsrc(const void *p, unsigned bytes) { return __builtin_amdgcn_make_buffer_rsrc((void *)p, 0, bytes, 0x00020000); }
template <int N> DEV void dma_tile128(rsrc_t r, unsigned soff, unsigned rstride, unsigned slot, int i0, int l) {
  const unsigned rr = l >> 4, cp = l & 15;
  const unsigned v0 = rr * rstride + ((((cp >> 1) ^ rr) << 1) | (cp & 1)) * 16;
  const unsigned v1 = rr * rstride + ((((cp >> 1) ^ (rr | 4)) << 1) | (cp & 1)) * 16;
  const unsigned sb = __builtin_amdgcn_readfirstlane(soff + i0 * 4 * rstride), lb = __builtin_amdgcn_readfirstlane(slot + i0 * 1024);
#pragma unroll
  for (int j = 0; j < N; ++j) dma16(r, (j & 2) ? v1 : v0, sb + j * 4 * rstride, lb + j * 1024);
}
template <int N> DEV void dma_tile64(rsrc_t r, unsigned soff, unsigned rstride, unsigned slot, int i0, int l) {
  const unsigned rr = l >> 3, cp = l & 7, f = (rr >> 1) & 1;
  const unsigned v0 = rr * rstride + ((((cp >> 1) ^ f) << 1) | (cp & 1)) * 16;
  const unsigned v1 = rr * rstride + ((((cp >> 1) ^ (f | 2)) << 1) | (cp & 1)) * 16;
  const unsigned sb = __builtin_amdgcn_readfirstlane(soff + i0 * 8 * rstride), lb = __builtin_amdgcn_readfirstlane(slot + i0 * 1024);
#pragma unroll
  for (int j = 0; j < N; ++j) dma16(r, (j & 1) ? v1 : v0, sb + j * 8 * rstride, lb + j * 1024);
}
DEV void dma_tilek(rsrc_t r, unsigned soff, unsigned slot, int i0, int l) {
  const unsigned rr = l >> 4, cp = l & 15;
  const unsigned sb = __builtin_amdgcn_readfirstlane(soff + i0 * 1024), lb = __builtin_amdgcn_readfirstlane(slot + i0 * 1024);
#pragma unroll
  for (int j = 0; j < 8; ++j) dma16(r, rr * 256 + ((cp ^ (4 * (j & 3) + rr)) * 16), sb + j * 1024, lb + j * 1024);
}
DEV v2u trread(LAS unsigned char *p) {
  return __builtin_bit_cast(v2u, __builtin_amdgcn_ds_read_tr16_b64_v4i16((LAS s4v *)p));
}
DEV void frags128(LAS unsigned char *slot, int m, int l, v4u (&f)[4]) {
  const unsigned g = l >> 4, q = (l & 15) >> 2, p = l & 3;
  const unsigned sw = q | ((g & 1) << 2);
  LAS unsigned char *a = slot + (8 * g + q) * 256 + ((m ^ sw) * 32) + p * 8;
#pragma unroll
  for (int s = 0; s < 4; ++s) {
    const v2u u0 = trread(a + s * 8192), u1 = trread(a + s * 8192 + 1024);
    f[s] = v4u{u0.x, u0.y, u1.x, u1.y};
  }
}
DEV void frags64(LAS unsigned char *slot, int m, int l, v4u (&f)[8]) {
  const unsigned g = l >> 4, q = (l & 15) >> 2, p = l & 3;
  const unsigned sw = ((q >> 1) & 1) | ((g & 1) << 1);
  LAS unsigned char *a = slot + (8 * g + q) * 128 + ((m ^ sw) * 32) + p * 8;
#pragma unroll
  for (int s = 0; s < 8; ++s) {
    const v2u u0 = trread(a + s * 4096), u1 = trread(a + s * 4096 + 512);
    f[s] = v4u{u0.x, u0.y, u1.x, u1.y};
  }
}
DEV v4u lds128(LAS unsigned char *p) { return *(LAS v4u *)p; }

DEV v4u ld128(rsrc_t r, unsigned v, unsigned s) { return __builtin_amdgcn_raw_buffer_load_b128(r, v, s, AUX_SC1); }
DEV v2u ld64(rsrc_t r, unsigned v, unsigned s) { return __builtin_amdgcn_raw_buffer_load_b64(r, v, s, AUX_SC1); }
DEV unsigned ld32(rsrc_t r, unsigned v, unsigned s) { return __builtin_amdgcn_raw_buffer_load_b32(r, v, s, AUX_SC1); }
DEV void st128(rsrc_t r, unsigned v, unsigned s, v4u d) { __builtin_amdgcn_raw_buffer_store_b128(d, r, v, s, AUX_SC1); }
DEV void st64(rsrc_t r, unsigned v, unsigned s, v2u d) { __builtin_amdgcn_raw_buffer_store_b64(d, r, v, s, AUX_SC1); }
DEV void st32(rsrc_t r, unsigned v, unsigned s, unsigned d) { __builtin_amdgcn_raw_buffer_store_b32(d, r, v, s, AUX_SC1); }
#define ALD128(d, r, v, s) asm volatile("buffer_load_dwordx4 %0, %1, %2, %3 offen sc1" : "=v"(d) : "v"(v), "s"(r), "s"(s) : "memory")
#define ALD64(d, r, v, s) asm volatile("buffer_load_dwordx2 %0, %1, %2, %3 offen sc1" : "=v"(d) : "v"(v), "s"(r), "s"(s) : "memory")
#define ALD32(d, r, v, s) asm volatile("buffer_load_dword %0, %1, %2, %3 offen sc1" : "=v"(d) : "v"(v), "s"(r), "s"(s) : "memory")
#define PLD64(d, r, v, s) asm volatile("buffer_load_dwordx2 %0, %1, %2, %3 offen" : "=v"(d) : "v"(v), "s"(r), "s"(s) : "memory")
#define PLD32(d, r, v, s) asm volatile("buffer_load_dword %0, %1, %2, %3 offen" : "=v"(d) : "v"(v), "s"(r), "s"(s) : "memory")
DEV bool any_bad(bool bad) { return __builtin_amdgcn_ballot_w64(bad) != 0ull; }
#define LIKELY_NOT(x) __builtin_expect((x), 0)

#define STAMP(i) do {} while (0)
#define STAMPW(i, wv) do {} while (0)

struct Ctx {
  rsrc_t rs;
  LAS unsigned char *L;
  int c, tid, w, l;
  unsigned reg;
  unsigned cap;
};
DEV unsigned region(unsigned c) { return WS_R + c * RS; }
DEV bool give_up(Ctx &C, unsigned &spin) {
  if (spin++ < C.cap) return false;
  C.cap = 0;
  return true;
}

DEV void row_stats(const v2u (&x)[4], float &mean, float &var) {
  float s1 = 0.f, s2 = 0.f, t1 = 0.f, t2 = 0.f;
#pragma unroll
  for (int i = 0; i < 4; ++i) {
    s1 = dot2(x[i].x, 0x3F803F80u, s1); s2 = dot2(x[i].x, x[i].x, s2);
    t1 = dot2(x[i].y, 0x3F803F80u, t1); t2 = dot2(x[i].y, x[i].y, t2);
  }
  s1 += t1; s2 += t2;
  wave_sum2(s1, s2);
  mean = s1 * (1.0f / DM);
  var = vmax(s2 * (1.0f / DM) - mean * mean, 0.f);
}
DEV bool row_bad(const v2u (&x)[4]) {
  const unsigned m = umax(umax(umax(x[0].x, x[0].y), umax(x[1].x, x[1].y)), umax(umax(x[2].x, x[2].y), umax(x[3].x, x[3].y)));
  return any_bad(m == SENT);
}
DEV void row_offsets(Ctx &C, unsigned roff, int kq, unsigned &voff, unsigned (&soff)[4]) {
  voff = (C.l >> 4) * 8 * RS + (C.l & 15) * 8;
#pragma unroll
  for (int i = 0; i < 4; ++i) soff[i] = region(C.w + 32 * ((kq + i) & 3)) + roff;
}
DEV void row_retry(Ctx &C, unsigned voff, const unsigned (&soff)[4], v2u (&x)[4]) {
  for (unsigned spin = 0; !give_up(C, spin);) {
    MEMFENCE();
#pragma unroll
    for (int i = 0; i < 4; ++i) x[i] = ld64(C.rs, voff, soff[i]);
    if (!row_bad(x)) break;
  }
}

DEV void stage_q_ln(Ctx &C, const v2u (&x)[4]) {
  float mean, var;
  const float x0 = lo16(x[0].x), x1 = hi16(x[0].x), x2 = lo16(x[0].y), x3 = hi16(x[0].y);
  row_stats(x, mean, var);
  const unsigned mv = pk(mean, var);
  const float mb = lo16(mv), sd = rbf(__builtin_amdgcn_sqrtf(rbf(hi16(mv) + EPS))), rs = __builtin_amdgcn_rcpf(sd);
  const unsigned d01 = pk(x0 - mb, x1 - mb), d23 = pk(x2 - mb, x3 - mb);
  v2u y = {pk(lo16(d01) * rs, hi16(d01) * rs), pk(lo16(d23) * rs, hi16(d23) * rs)};
  *(LAS v2u *)(C.L + L_Y + C.w * YS + C.l * 8) = y;
}
DEV f4 mm8(Ctx &C, const v4u (&wf)[8]) {
  LAS unsigned char *yb = C.L + L_Y + (C.l & 7) * YS + (C.l >> 4) * 16;
  v4u b[8];
#pragma unroll
  for (int s = 0; s < 8; ++s) b[s] = lds128(yb + s * 64);
  SCHED();
  f4 acc = {0.f, 0.f, 0.f, 0.f};
#pragma unroll
  for (int s = 0; s < 8; ++s) acc = mfma(wf[s], b[s], acc);
  return acc;
}
DEV f4 mm4(Ctx &C, unsigned base, const v4u (&wf)[4]) {
  LAS unsigned char *ab = C.L + base + (C.l & 7) * AS + (C.l >> 4) * 16;
  v4u b[4];
#pragma unroll
  for (int s = 0; s < 4; ++s) b[s] = lds128(ab + s * 64);
  SCHED();
  f4 acc = {0.f, 0.f, 0.f, 0.f};
#pragma unroll
  for (int s = 0; s < 4; ++s) acc = mfma(wf[s], b[s], acc);
  return acc;
}
template <int LAYER>
DEV void stage_q_mm(Ctx &C, const v4u (&wf)[8], unsigned taglo, unsigned taghi) {
  const int l = C.l, w = C.w;
  if (w < 4) {
    const f4 acc = mm8(C, wf);
    if constexpr (LAYER == 0) {
      const bool up = (l & 8) != 0;
      const float a2 = dppm<0x128>(acc.z), a3 = dppm<0x128>(acc.w);
      const float v0 = up ? a2 : acc.x, v1 = up ? a3 : acc.y;
      const v4u o = {taglo, (__float_as_uint(v0) & 0xFFFFFF00u) | taghi, taglo, (__float_as_uint(v1) & 0xFFFFFF00u) | taghi};
      st128(C.rs, (l & 7) * 128 + ((l >> 3) & 1) * 64 + (l >> 4) * 16, WS_Q0 + C.c * Q0S + w * 1024, o);
    } else {
      if ((l & 15) < 8) st128(C.rs, (l & 15) * 64 + (l >> 4) * 16, C.reg + R_Q1 + w * 512, nosent4(acc));
    }
  }
}

struct Frags { v4u wqw1[8]; v4u k[4], v[4], wo[4], w2[4]; };

template <int LAYER>
DEV void layer(Ctx &C, const Params &p, Frags &F, unsigned b1v, v2u b2v, v2u &xres, unsigned taglo, unsigned taghi,
               unsigned genlo, unsigned genhi) {
  const int c = C.c, w = C.w, l = C.l, tid = C.tid, g = l >> 4;
  LAS unsigned char *L = C.L;
  const int xc = c & 7, r = c >> 3;
  constexpr unsigned R_OB = LAYER ? R_OB1 : R_OB0, R_LSE = LAYER ? R_LSE1 : R_LSE0, R_XP = LAYER ? R_XP1 : R_XP0;
  constexpr unsigned R_X1 = LAYER ? R_X11 : R_X10, R_HP = LAYER ? R_HP1 : R_HP0, R_X2P = LAYER ? R_X2P1 : R_X2P0;
  const unsigned kvoff = ((xc * 4 + (r >> 3)) * SK + (SK - WIN) + 128 * (r & 7)) * 256;
  const unsigned blk = (l & 15) * 64 + g * 16;

  if constexpr (LAYER == 0) {
    if (w < 4) {
      NAP(D_Q0);
      const unsigned voff = (l >> 5) * Q0S + ((l >> 3) & 3) * 1024 + (l & 1) * 64 + ((l & 7) >> 1) * 16;
      const unsigned soff = WS_Q0 + 2 * (8 * (r >> 3) + w) * Q0S + xc * 128;
      const unsigned vchk = l * Q0S, schk = WS_Q0 + 64 * w * Q0S;
      v4u v[8]; v2u chk;
#pragma unroll
      for (int i = 0; i < 8; ++i) ALD128(v[i], C.rs, voff, soff + (i & 3) * 64 * Q0S + (i >> 2) * 8 * Q0S);
      ALD64(chk, C.rs, vchk, schk);
      dma_tile128<8>(mkrsrc(p.vc, 8 * 4 * SK * 256), kvoff, 256, L_S1, 8 * w, l);
      asm volatile("s_waitcnt vmcnt(8)" : "+v"(v[0]), "+v"(v[1]), "+v"(v[2]), "+v"(v[3]), "+v"(v[4]), "+v"(v[5]), "+v"(v[6]), "+v"(v[7]), "+v"(chk) :: "memory");
      auto bad = [&]() {
        bool b = chk.x != taglo || (chk.y & 0xFF) != taghi;
#pragma unroll
        for (int i = 0; i < 8; ++i) b |= v[i].x != taglo || v[i].z != taglo || (v[i].y & 0xFF) != taghi || (v[i].w & 0xFF) != taghi;
        return any_bad(b);
      };
      if (LIKELY_NOT(bad())) {
        for (unsigned spin = 0; !give_up(C, spin);) {
          MEMFENCE();
#pragma unroll
          for (int i = 0; i < 8; ++i) v[i] = ld128(C.rs, voff, soff + (i & 3) * 64 * Q0S + (i >> 2) * 8 * Q0S);
          chk = ld64(C.rs, vchk, schk);
          if (!bad()) break;
        }
      }
#pragma unroll
      for (int hh = 0; hh < 2; ++hh) {
        float q0 = 0.f, q1 = 0.f;
#pragma unroll
        for (int i = 0; i < 4; ++i) { q0 += __uint_as_float(v[4 * hh + i].y & 0xFFFFFF00u); q1 += __uint_as_float(v[4 * hh + i].w & 0xFFFFFF00u); }
        *(LAS unsigned *)(L + L_Q + (w + 4 * hh) * AS + l * 4) = pk(q0, q1);
      }
    } else {
      VMWAIT(0);
    }
  } else {
    const unsigned voff = (l >> 5) * RS + ((l >> 3) & 3) * 512 + (l & 7) * 8;
    const unsigned soff = region(2 * (8 * (r >> 3) + w)) + R_Q1 + xc * 64;
    NAP(D_Q1);
    v2u v[4];
#pragma unroll
    for (int i = 0; i < 4; ++i) v[i] = ld64(C.rs, voff, soff + i * 64 * RS);
    if (LIKELY_NOT(row_bad(v))) {
      for (unsigned spin = 0; !give_up(C, spin);) {
        MEMFENCE();
#pragma unroll
        for (int i = 0; i < 4; ++i) v[i] = ld64(C.rs, voff, soff + i * 64 * RS);
        if (!row_bad(v)) break;
      }
    }
    const float q0 = (__uint_as_float(v[0].x) + __uint_as_float(v[1].x)) + (__uint_as_float(v[2].x) + __uint_as_float(v[3].x));
    const float q1 = (__uint_as_float(v[0].y) + __uint_as_float(v[1].y)) + (__uint_as_float(v[2].y) + __uint_as_float(v[3].y));
    *(LAS unsigned *)(L + L_Q + w * AS + l * 4) = pk(q0, q1);
  }
  STAMP(1);
  BAR();
  {
    const int hh = l & 7;
    const bool low = (l & 15) < 8;
    if constexpr (LAYER == 0) {
      if (c == 0 && tid == 0) st64(C.rs, 0, WS_GEN, v2u{genlo, genhi});
      LAS unsigned char *kb = L + L_S2 + (16 * w + (l & 15)) * 256;
#pragma unroll
      for (int s = 0; s < 4; ++s) F.k[s] = lds128(kb + (((4 * s + g) ^ (l & 15)) * 16));
    }
    const f4 acc = mm4(C, L_Q, F.k);
    const unsigned a01 = pk(acc.x, acc.y), a23 = pk(acc.z, acc.w);
    const unsigned s01 = pk(lo16(a01) * QK_SCALE, hi16(a01) * QK_SCALE), s23 = pk(lo16(a23) * QK_SCALE, hi16(a23) * QK_SCALE);
    const float sc0 = lo16(s01), sc1 = hi16(s01), sc2 = lo16(s23), sc3 = hi16(s23);
    const float wm = xrow_max(vmax(vmax3(sc0, sc1, sc2), sc3));
    if (l < 8) *(LAS float *)(L + L_M + l * 32 + w * 4) = wm;
    BAR();
    const f4 m0 = *(LAS f4 *)(L + L_M + hh * 32), m1 = *(LAS f4 *)(L + L_M + hh * 32 + 16);
    const float M = vmax3(vmax3(m0.x, m0.y, m0.z), vmax3(m0.w, m1.x, m1.y), vmax(m1.z, m1.w));
    const float Ml = M * LOG2E;
    const float e0 = __builtin_amdgcn_exp2f(fmaf(sc0, LOG2E, -Ml)), e1 = __builtin_amdgcn_exp2f(fmaf(sc1, LOG2E, -Ml));
    const float e2 = __builtin_amdgcn_exp2f(fmaf(sc2, LOG2E, -Ml)), e3 = __builtin_amdgcn_exp2f(fmaf(sc3, LOG2E, -Ml));
    if (low) *(LAS v2u *)(L + L_P + hh * AS + (16 * w + 4 * g) * 2) = v2u{pk(e0, e1), pk(e2, e3)};
    const float ls = xrow_sum((e0 + e1) + (e2 + e3));
    if (l < 8) *(LAS float *)(L + L_L + l * 32 + w * 4) = ls;
    if constexpr (LAYER == 0) {
      dma_tile128<4>(mkrsrc(p.wo, 4096 * DM * 2), (128 * (xc + 8 * (r >> 3)) * DM + 128 * (r & 7)) * 2, DM * 2, L_S2, 4 * w, l);
      if (w < 4) VMWAIT(4);
    }
    BAR();
    if constexpr (LAYER == 0) frags128(L + L_S1, w, l, F.v);
    const f4 l0 = *(LAS f4 *)(L + L_L + hh * 32), l1 = *(LAS f4 *)(L + L_L + hh * 32 + 16);
    const f4 o = mm4(C, L_P, F.v);
    const float Ls = ((l0.x + l0.y) + (l0.z + l0.w)) + ((l1.x + l1.y) + (l1.z + l1.w));
    const float inv = __builtin_amdgcn_rcpf(Ls);
    if (low) *(LAS v2u *)(L + L_O + hh * AS + (16 * w + 4 * g) * 2) = v2u{pk(o.x * inv, o.y * inv), pk(o.z * inv, o.w * inv)};
    if (w == 2 && l < 8) st32(C.rs, l * 4, C.reg + R_LSE, nosentf(Ml + __builtin_amdgcn_logf(Ls)));
    BAR();
    if (w < 2) {
      const v4u v = lds128(L + L_O + (tid >> 4) * AS + (tid & 15) * 16);
      st128(C.rs, tid * 16, C.reg + R_OB, v4u{nosent(v.x), nosent(v.y), nosent(v.z), nosent(v.w)});
    }
  }
  STAMP(2);
  if constexpr (LAYER == 0) {
#pragma unroll
    for (int i = 0; i < 3; ++i) {
      const int j = w + 8 * i;
      if (j < (int)R_BCLRN) st128(C.rs, l * 16, C.reg + R_BCLR0 + j * 1024, v4u{SENT, SENT, SENT, SENT});
    }
  }
  {
    const int h = xc + 8 * (r >> 3);
    const unsigned base = region(w + 64 * (h >> 3));
    const unsigned soff = base + R_OB + (h & 7) * 256, soffl = base + R_LSE + (h & 7) * 4;
    const unsigned voff = l * 4, voffl = (l & 7) * 8 * RS;
    unsigned v[8], lse;
    NAP(LAYER ? D_O1 : D_O0);
    if constexpr (LAYER == 0) {
      ALD32(lse, C.rs, voffl, soffl);
#pragma unroll
      for (int s = 0; s < 8; ++s) ALD32(v[s], C.rs, voff, soff + s * 8 * RS);
      if (c < 224) {
        dma_tile64<4>(mkrsrc(p.w1, DM * DFF * 2), (256 * (c / 56) * DFF + 64 * (c % 56)) * 2, DFF * 2, L_S3, 4 * w, l);
        VMWAIT(4);
      } else {
        VMWAIT(0);
      }
      asm volatile("" : "+v"(v[0]), "+v"(v[1]), "+v"(v[2]), "+v"(v[3]), "+v"(v[4]), "+v"(v[5]), "+v"(v[6]), "+v"(v[7]), "+v"(lse));
    } else {
      lse = ld32(C.rs, voffl, soffl);
#pragma unroll
      for (int s = 0; s < 8; ++s) v[s] = ld32(C.rs, voff, soff + s * 8 * RS);
    }
    auto bad = [&]() {
      return any_bad(umax(umax(umax(umax(v[0], v[1]), umax(v[2], v[3])), umax(umax(v[4], v[5]), umax(v[6], v[7]))), lse) == SENT);
    };
    if (LIKELY_NOT(bad())) {
      for (unsigned spin = 0; !give_up(C, spin);) {
        MEMFENCE();
        lse = ld32(C.rs, voffl, soffl);
#pragma unroll
        for (int s = 0; s < 8; ++s) v[s] = ld32(C.rs, voff, soff + s * 8 * RS);
        if (!bad()) break;
      }
    }
    const float ls = __uint_as_float(lse);
    float mx = vmax(ls, dppm<0xB1>(ls)); mx = vmax(mx, dppm<0x4E>(mx)); mx = vmax(mx, dppm<0x141>(mx));
    const float e = __builtin_amdgcn_exp2f(ls - mx);
    float sm = e + dppm<0xB1>(e); sm += dppm<0x4E>(sm); sm += dppm<0x141>(sm);
    const float wt = e * __builtin_amdgcn_rcpf(sm);
    float o0 = 0.f, o1 = 0.f;
#pragma unroll
    for (int s = 0; s < 8; ++s) {
      const float ws = rdlane(wt, s);
      o0 = fmaf(ws, lo16(v[s]), o0); o1 = fmaf(ws, hi16(v[s]), o1);
    }
    *(LAS unsigned *)(L + L_A + w * AS + l * 4) = pk(o0, o1);
    STAMP(3);
    BAR();
    if constexpr (LAYER == 0) frags128(L + L_S2, w, l, F.wo);
    const f4 acc = mm4(C, L_A, F.wo);
    if ((l & 15) < 8) st128(C.rs, blk, C.reg + R_XP + w * 512, nosent4(acc));
  }
  STAMP(4);
  const int t3 = c & 7, cc = (c >> 3) & 15;
  const bool reducer = c < 128 && w == 7;
  const unsigned rvoff = g * RS + ((l >> 2) & 3) * 512 + (l & 3) * 16;
  if (reducer) {
    const unsigned soff = region(8 * (cc >> 1)) + R_XP + 4 * (cc & 1) * 512 + t3 * 64;
    NAP(LAYER ? D_XP1 : D_XP0);
    v4u v[8];
#pragma unroll
    for (int i = 0; i < 8; ++i) v[i] = ld128(C.rs, rvoff, soff + (i & 1) * 4 * RS + (i >> 1) * 64 * RS);
    auto bad = [&]() {
      unsigned m = 0;
#pragma unroll
      for (int i = 0; i < 8; ++i) m = umax(m, umax4(v[i]));
      return any_bad(m == SENT);
    };
    if (LIKELY_NOT(bad())) {
      for (unsigned spin = 0; !give_up(C, spin);) {
        MEMFENCE();
#pragma unroll
        for (int i = 0; i < 8; ++i) v[i] = ld128(C.rs, rvoff, soff + (i & 1) * 4 * RS + (i >> 1) * 64 * RS);
        if (!bad()) break;
      }
    }
    f4 a = __builtin_bit_cast(f4, v[0]);
#pragma unroll
    for (int i = 1; i < 8; ++i) a += __builtin_bit_cast(f4, v[i]);
    const unsigned a01 = pk(xrow_sum(a.x), xrow_sum(a.y)), a23 = pk(xrow_sum(a.z), xrow_sum(a.w));
    xres = v2u{pk(lo16(xres.x) + lo16(a01), hi16(xres.x) + hi16(a01)), pk(lo16(xres.y) + lo16(a23), hi16(xres.y) + hi16(a23))};
    if (l < 16) st64(C.rs, l * 8, C.reg + R_X1, v2u{nosent(xres.x), nosent(xres.y)});
    STAMPW(5, 7);
    NAP(LAYER ? D_X1A1 : D_X1A0);
  } else {
    NAP(LAYER ? D_X1B1 : D_X1B0);
  }
  const int kr = xc + 8 * (r >> 3);
  const bool act5 = kr < 28;
  {
    const bool act = c < 224;
    const int kq = act ? c / 56 : 0;
    v2u x[4];
    unsigned voff, soff[4];
    row_offsets(C, R_X1, kq, voff, soff);
    if constexpr (LAYER == 0) {
#pragma unroll
      for (int i = 0; i < 4; ++i) ALD64(x[i], C.rs, voff, soff[i]);
      if (act5) {
        dma_tile128<4>(mkrsrc(p.w2, DFF * DM * 2), (128 * kr * DM + 128 * (r & 7)) * 2, DM * 2, L_S0, 4 * w, l);
        VMWAIT(4);
      } else {
        VMWAIT(0);
      }
      asm volatile("" : "+v"(x[0]), "+v"(x[1]), "+v"(x[2]), "+v"(x[3]));
    } else {
#pragma unroll
      for (int i = 0; i < 4; ++i) x[i] = ld64(C.rs, voff, soff[i]);
    }
    if (LIKELY_NOT(row_bad(x))) row_retry(C, voff, soff, x);
    STAMP(6);
    float mean, var;
    const float x0 = lo16(x[0].x), x1 = hi16(x[0].x), x2 = lo16(x[0].y), x3 = hi16(x[0].y);
    row_stats(x, mean, var);
    const float r1 = __builtin_amdgcn_rsqf(var + EPS), v2 = var * r1 * r1, rr = r1 * __builtin_amdgcn_rsqf(v2 + EPS);
    const v2u y = {pk((x0 - mean) * rr, (x1 - mean) * rr), pk((x2 - mean) * rr, (x3 - mean) * rr)};
    *(LAS v2u *)(L + L_Y + w * YS + l * 8) = y;
    BAR();
    if (w >= 4 && act) {
      if constexpr (LAYER == 0) frags64(L + L_S3, w - 4, l, F.wqw1);
      const f4 acc = mm8(C, F.wqw1);
      if ((l & 15) < 8) st128(C.rs, blk, C.reg + R_HP + (w - 4) * 512, nosent4(acc));
    }
  }
  STAMPW(7, 4);
  {
    if (act5) {
      const unsigned soff = region(2 * kr + (w >> 2)) + R_HP + (w & 3) * 512, voff = l * 8;
      NAP(LAYER ? D_HP1 : D_HP0);
      v2u v[4];
#pragma unroll
      for (int i = 0; i < 4; ++i) v[i] = ld64(C.rs, voff, soff + i * 56 * RS);
      if (LIKELY_NOT(row_bad(v))) {
        for (unsigned spin = 0; !give_up(C, spin);) {
          MEMFENCE();
#pragma unroll
          for (int i = 0; i < 4; ++i) v[i] = ld64(C.rs, voff, soff + i * 56 * RS);
          if (!row_bad(v)) break;
        }
      }
      if constexpr (LAYER == 0) VMWAIT(0);
      const float p0 = (__uint_as_float(v[0].x) + __uint_as_float(v[1].x)) + (__uint_as_float(v[2].x) + __uint_as_float(v[3].x));
      const float p1 = (__uint_as_float(v[0].y) + __uint_as_float(v[1].y)) + (__uint_as_float(v[2].y) + __uint_as_float(v[3].y));
      const unsigned pp = pk(p0, p1), hp = pk(lo16(pp) + lo16(b1v), hi16(pp) + hi16(b1v));
      const float h0 = lo16(hp), h1 = hi16(hp);
      constexpr float GC = -2.0f * 0.7978845608028654f * LOG2E;
      const float u0 = h0 * fmaf(GC * 0.044715f * h0, h0, GC), u1 = h1 * fmaf(GC * 0.044715f * h1, h1, GC);
      const float g0 = h0 * __builtin_amdgcn_rcpf(1.0f + __builtin_amdgcn_exp2f(u0));
      const float g1 = h1 * __builtin_amdgcn_rcpf(1.0f + __builtin_amdgcn_exp2f(u1));
      *(LAS unsigned *)(L + L_A + (l >> 3) * AS + (16 * w + 2 * (l & 7)) * 2) = pk(g0, g1);
    }
    STAMP(8);
    BAR();
    if (act5) {
      if constexpr (LAYER == 0) frags128(L + L_S0, w, l, F.w2);
      const f4 acc = mm4(C, L_A, F.w2);
      if ((l & 15) < 8) st128(C.rs, blk, C.reg + R_X2P + w * 512, nosent4(acc));
    }
  }
  STAMP(9);
  if (reducer) {
    const unsigned soff = region(8 * (cc >> 1)) + R_X2P + 4 * (cc & 1) * 512 + t3 * 64;
    NAP(LAYER ? D_X2P1 : D_X2P0);
    v4u v[7];
#pragma unroll
    for (int i = 0; i < 7; ++i) v[i] = ld128(C.rs, rvoff, soff + (i & 1) * 4 * RS + (i >> 1) * 64 * RS);
    auto bad = [&]() {
      unsigned m = 0;
#pragma unroll
      for (int i = 0; i < 7; ++i) m = umax(m, umax4(v[i]));
      return any_bad(m == SENT);
    };
    if (LIKELY_NOT(bad())) {
      for (unsigned spin = 0; !give_up(C, spin);) {
        MEMFENCE();
#pragma unroll
        for (int i = 0; i < 7; ++i) v[i] = ld128(C.rs, rvoff, soff + (i & 1) * 4 * RS + (i >> 1) * 64 * RS);
        if (!bad()) break;
      }
    }
    f4 a = __builtin_bit_cast(f4, v[0]);
#pragma unroll
    for (int i = 1; i < 7; ++i) a += __builtin_bit_cast(f4, v[i]);
    const unsigned a01 = pk(xrow_sum(a.x), xrow_sum(a.y)), a23 = pk(xrow_sum(a.z), xrow_sum(a.w));
    const unsigned z01 = pk(lo16(a01) + lo16(b2v.x), hi16(a01) + hi16(b2v.x)), z23 = pk(lo16(a23) + lo16(b2v.y), hi16(a23) + hi16(b2v.y));
    xres = v2u{pk(lo16(xres.x) + lo16(z01), hi16(xres.x) + hi16(z01)), pk(lo16(xres.y) + lo16(z23), hi16(xres.y) + hi16(z23))};
    if constexpr (LAYER == 0) {
      if (l < 16) st64(C.rs, l * 8, C.reg + R_X2, v2u{nosent(xres.x), nosent(xres.y)});
    } else {
      if (l < 16) *(GAS v2u *)(p.out + t3 * DM + 64 * cc + 4 * l) = xres;
    }
    STAMPW(10, 7);
    if constexpr (LAYER == 0) NAP(D_X2A0);
  } else {
    if constexpr (LAYER == 0) NAP(D_X2B0);
  }
  if constexpr (LAYER == 0) {
    v2u x[4];
    unsigned voff, soff[4];
    row_offsets(C, R_X2, c >> 6, voff, soff);
#pragma unroll
    for (int i = 0; i < 4; ++i) x[i] = ld64(C.rs, voff, soff[i]);
    if (LIKELY_NOT(row_bad(x))) row_retry(C, voff, soff, x);
    STAMP(11);
    stage_q_ln(C, x);
    BAR();
    stage_q_mm<1>(C, F.wqw1, 0, 0);
    STAMP(12);
  }
}

__global__ void __launch_bounds__(NT, 2) fused_decode_kernel(Params p) {
  __shared__ __attribute__((aligned(16))) unsigned char smem[LDS_BYTES];
  Ctx C;
  C.L = (LAS unsigned char *)smem;
  C.c = blockIdx.x; C.tid = threadIdx.x; C.l = threadIdx.x & 63;
  C.w = __builtin_amdgcn_readfirstlane(threadIdx.x >> 6);
  C.rs = mkrsrc(p.ws, WS_BYTES);
  C.reg = region(C.c);
  C.cap = SPIN_CAP;
  const int c = C.c, w = C.w, l = C.l, tid = C.tid, xc = c & 7, r = c >> 3;
  STAMP(0);
  v2u x[4], gen, b2v, xres;
  unsigned b1v, warm;
  {
    const rsrc_t rx = mkrsrc(p.x, 8 * DM * 2);
    const int kq = c >> 6;
#pragma unroll
    for (int i = 0; i < 4; ++i) PLD64(x[i], rx, l * 8, w * 2048 + ((kq + i) & 3) * 512);
    ALD64(gen, C.rs, 0, WS_GEN);
    if (w < 4) dma_tile64<8>(mkrsrc(p.wq, DM * 4096 * 2), (256 * (c >> 6) * 4096 + 64 * (c & 63)) * 2, 8192, L_S0, 8 * w, l);
    const int kr = xc + 8 * (r >> 3), kra = kr < 28 ? kr : 0, cc = (c >> 3) & 15;
    PLD32(b1v, mkrsrc(p.b1, DFF * 2), (l & 7) * 4, (128 * kra + 16 * w) * 2);
    PLD64(b2v, mkrsrc(p.b2, DM * 2), (l & 15) * 8, 64 * cc * 2);
    PLD64(xres, rx, (l & 15) * 8, ((c & 7) * DM + 64 * cc) * 2);
    unsigned long long pc;
    asm volatile("s_getpc_b64 %0" : "=s"(pc));
    unsigned line = r * 8 + w;
    line = line < CODE_LINES ? line : CODE_LINES - 1;
    asm volatile("global_load_dword %0, %1, %2" : "=v"(warm) : "v"(line * 128), "s"(pc) : "memory");
  }
  st128(C.rs, l * 16, C.reg + w * 1024, v4u{SENT, SENT, SENT, SENT});
  if (w < 4) asm volatile("s_waitcnt vmcnt(13)" ::: "memory"); else asm volatile("s_waitcnt vmcnt(5)" ::: "memory");
  asm volatile("" : "+v"(gen), "+v"(x[0]), "+v"(x[1]), "+v"(x[2]), "+v"(x[3]));
  const unsigned glo = __builtin_amdgcn_readfirstlane(gen.x), ghi = __builtin_amdgcn_readfirstlane(gen.y);
  const unsigned genlo = glo + 1, genhi = ghi + (genlo == 0 ? 1 : 0);
  const unsigned taglo = genlo, taghi = genhi & 0xFF;
  Frags F;
#pragma unroll
  for (int s = 0; s < 8; ++s) asm volatile("" : "=v"(F.wqw1[s]));
  STAMP(1);
  stage_q_ln(C, x);
  if (w < 4) {
    VMWAIT(0);
  } else {
    const unsigned kvoff = ((xc * 4 + (r >> 3)) * SK + (SK - WIN) + 128 * (r & 7)) * 256;
    dma_tilek(mkrsrc(p.kc, 8 * 4 * SK * 256), kvoff, L_S2, 8 * (w - 4), l);
    VMWAIT(8);
  }
  asm volatile("" : "+v"(b1v), "+v"(b2v), "+v"(xres), "+v"(warm));
  BAR();
  STAMP(2);
  if (w < 4) frags64(C.L + L_S0, w, l, F.wqw1);
  stage_q_mm<0>(C, F.wqw1, taglo, taghi);
  STAMP(3);
  layer<0>(C, p, F, b1v, b2v, xres, taglo, taghi, genlo, genhi);
  layer<1>(C, p, F, b1v, b2v, xres, taglo, taghi, genlo, genhi);
}

}

extern "C" void kernel_launch(void *const *d_in, const int *in_sizes, int n_in, void *d_out, int out_size, void *d_ws,
                              size_t ws_size, hipStream_t stream) {
  Params p;
  p.x = (const u16 *)d_in[0]; p.kc = (const u16 *)d_in[1]; p.vc = (const u16 *)d_in[2];
  p.wq = (const u16 *)d_in[3]; p.wo = (const u16 *)d_in[4];
  p.w1 = (const u16 *)d_in[6]; p.b1 = (const u16 *)d_in[7]; p.w2 = (const u16 *)d_in[8]; p.b2 = (const u16 *)d_in[9];
  p.out = (u16 *)d_out; p.ws = (unsigned char *)d_ws;
  hipLaunchKernelGGL(fused_decode_kernel, dim3(NCU), dim3(NT), 0, stream, p);
}
```

```cpp
#include <hip/hip_runtime.h>
#include <stdint.h>

namespace {
typedef unsigned short u16;
typedef unsigned v4u __attribute__((ext_vector_type(4)));
typedef unsigned v2u __attribute__((ext_vector_type(2)));
typedef float f4 __attribute__((ext_vector_type(4)));
typedef float f2 __attribute__((ext_vector_type(2)));
typedef short s8v __attribute__((ext_vector_type(8)));
typedef short s4v __attribute__((ext_vector_type(4)));
typedef __bf16 bf2 __attribute__((ext_vector_type(2)));
typedef __amdgpu_buffer_rsrc_t rsrc_t;
#define LAS __attribute__((address_space(3)))
#define GAS __attribute__((address_space(1)))
#define DEV __device__ __forceinline__

constexpr int NCU = 256, NT = 512;
constexpr int DM = 1024, SK = 4096, WIN = 1024, DFF = 3584;

constexpr unsigned CH(unsigned win, unsigned chunk) { return win * (2u << 20) + chunk * 16384; }
constexpr unsigned C_OB0 = CH(0, 0), C_OB1 = CH(0, 32), C_XP0 = CH(0, 64), C_X2P0 = CH(0, 96), C_XP1 = CH(1, 0), C_X2P1 = CH(1, 32);
constexpr unsigned C_LSE0 = CH(1, 96), C_LSE1 = CH(1, 100), C_X10 = CH(1, 104), C_X2 = CH(1, 106), C_X11 = CH(1, 108);
constexpr unsigned C_P2 = CH(1, 64);
constexpr unsigned C_PROBE = CH(1, 112);
constexpr unsigned WS_GEN = 4u << 20;
constexpr unsigned WS_Q0 = WS_GEN + 4096, Q0S = 4096 + 128;
constexpr unsigned WS_VOTE = WS_Q0 + NCU * Q0S;
constexpr unsigned WS_BYTES = WS_VOTE + 32 * 128;
constexpr unsigned SENT = 0xFFFFFFFFu;
constexpr int AUX_SC1 = 16;

constexpr unsigned L_S0 = 0, L_S1 = 32768, L_S2 = 65536, L_S3 = 98304;
constexpr unsigned YS = 544, AS = 288;
constexpr unsigned L_Y = 131072, L_Q = L_Y + 8 * YS, L_P = L_Q + 8 * AS, L_A = L_P + 8 * AS;
constexpr unsigned L_M = L_A + 8 * AS, L_XR = L_M + 256, L_PR = L_XR + 256, L_VC = L_PR + 16, L_END = L_VC + 16;
constexpr unsigned LDS_BYTES = 147456;

constexpr float EPS = 1e-5f, LOG2E = 1.4426950408889634f, QK_SCALE = 0.08838834764831845f;
constexpr unsigned SPIN_CAP = 1u << 16;

constexpr unsigned CODE_LINES = 195;
constexpr int D_Q0 = 14, D_O0 = 52, D_XP0 = 36, D_W2 = 8, D_X10 = 64, D_HP0 = 17, D_X2P0 = 15, D_X2 = 56;
constexpr int D_Q1 = 15, D_O1 = 19, D_XP1 = 14, D_X11 = 45, D_HP1 = 14, D_X2P1 = 13;

struct Params {
  const u16 *x, *kc, *vc, *wq, *wo, *w1, *b1, *w2, *b2;
  u16 *out;
  unsigned char *ws;
};

DEV float lo16(unsigned v) { return __uint_as_float(v << 16); }
DEV float hi16(unsigned v) { return __uint_as_float(v & 0xffff0000u); }
DEV unsigned pk(float a, float b) { f2 v = {a, b}; return __builtin_bit_cast(unsigned, __builtin_convertvector(v, bf2)); }
DEV float rbf(float a) { return lo16(pk(a, a)); }
DEV unsigned nosent(unsigned v) { return v < 0xFFFFFFFEu ? v : 0xFFFFFFFEu; }
DEV unsigned nosentf(float v) { return nosent(__float_as_uint(v)); }
template <int K> DEV unsigned coded(float v) { return (__float_as_uint(v) & ~15u) | (15u ^ (1u << K)); }
template <int K> DEV v4u coded4(f4 a) { return v4u{coded<K>(a.x), coded<K>(a.y), coded<K>(a.z), coded<K>(a.w)}; }
DEV float dot2(unsigned a, unsigned b, float c) {
  return __builtin_amdgcn_fdot2_f32_bf16(__builtin_bit_cast(bf2, a), __builtin_bit_cast(bf2, b), c, false);
}
DEV f4 mfma(v4u a, v4u b, f4 c) {
  return __builtin_amdgcn_mfma_f32_16x16x32_bf16(__builtin_bit_cast(s8v, a), __builtin_bit_cast(s8v, b), c, 0, 0, 0);
}
DEV float vmax(float a, float b) { float r; asm("v_max_f32 %0, %1, %2" : "=v"(r) : "v"(a), "v"(b)); return r; }
DEV float vmax3(float a, float b, float c) { float r; asm("v_max3_f32 %0, %1, %2, %3" : "=v"(r) : "v"(a), "v"(b), "v"(c)); return r; }
template <int CTRL> DEV float dppm(float v) {
  return __builtin_bit_cast(float, __builtin_amdgcn_mov_dpp(__builtin_bit_cast(int, v), CTRL, 0xf, 0xf, true));
}
DEV float xrow_max(float x) {
  auto s = __builtin_amdgcn_permlane16_swap(__float_as_uint(x), __float_as_uint(x), false, false);
  x = vmax(__uint_as_float(s[0]), __uint_as_float(s[1]));
  auto t = __builtin_amdgcn_permlane32_swap(__float_as_uint(x), __float_as_uint(x), false, false);
  return vmax(__uint_as_float(t[0]), __uint_as_float(t[1]));
}
DEV float xrow_sum(float x) {
  auto s = __builtin_amdgcn_permlane16_swap(__float_as_uint(x), __float_as_uint(x), false, false);
  x = __uint_as_float(s[0]) + __uint_as_float(s[1]);
  auto t = __builtin_amdgcn_permlane32_swap(__float_as_uint(x), __float_as_uint(x), false, false);
  return __uint_as_float(t[0]) + __uint_as_float(t[1]);
}
DEV unsigned umax(unsigned a, unsigned b) { return a > b ? a : b; }
DEV unsigned umax4(v4u v) { return umax(umax(v.x, v.y), umax(v.z, v.w)); }
#define BAR() asm volatile("s_waitcnt lgkmcnt(0)\n\ts_barrier" ::: "memory")
#define VMWAIT(n) asm volatile("s_waitcnt vmcnt(" #n ")" ::: "memory")
#define MEMFENCE() asm volatile("" ::: "memory")
#define SCHED() __builtin_amdgcn_sched_barrier(0)
template <int N> DEV void nap() { if constexpr (N > 0) __builtin_amdgcn_s_sleep(N); }

DEV void dma16(rsrc_t r, unsigned voff, unsigned soff, unsigned ldst) {
  asm volatile("s_mov_b32 m0, %0\n\ts_nop 3\n\tbuffer_load_dwordx4 %1, %2, %3 offen nt lds"
               :: "s"(ldst), "v"(voff), "s"(r), "s"(soff) : "memory", "m0");
}
DEV rsrc_t mkrsrc(const void *p, unsigned bytes) { return __builtin_amdgcn_make_buffer_rsrc((void *)p, 0, bytes, 0x00020000); }
template <int N> DEV void dma_tile128(rsrc_t r, unsigned soff, unsigned rstride, unsigned slot, int i0, int l) {
  const unsigned rr = l >> 4, cp = l & 15;
  const unsigned v0 = rr * rstride + ((((cp >> 1) ^ rr) << 1) | (cp & 1)) * 16;
  const unsigned v1 = rr * rstride + ((((cp >> 1) ^ (rr | 4)) << 1) | (cp & 1)) * 16;
  const unsigned sb = __builtin_amdgcn_readfirstlane(soff + i0 * 4 * rstride), lb = __builtin_amdgcn_readfirstlane(slot + i0 * 1024);
#pragma unroll
  for (int j = 0; j < N; ++j) dma16(r, (j & 2) ? v1 : v0, sb + j * 4 * rstride, lb + j * 1024);
}
template <int N> DEV void dma_tile64(rsrc_t r, unsigned soff, unsigned rstride, unsigned slot, int i0, int l) {
  const unsigned rr = l >> 3, cp = l & 7, f = (rr >> 1) & 1;
  const unsigned v0 = rr * rstride + ((((cp >> 1) ^ f) << 1) | (cp & 1)) * 16;
  const unsigned v1 = rr * rstride + ((((cp >> 1) ^ (f | 2)) << 1) | (cp & 1)) * 16;
  const unsigned sb = __builtin_amdgcn_readfirstlane(soff + i0 * 8 * rstride), lb = __builtin_amdgcn_readfirstlane(slot + i0 * 1024);
#pragma unroll
  for (int j = 0; j < N; ++j) dma16(r, (j & 1) ? v1 : v0, sb + j * 8 * rstride, lb + j * 1024);
}
DEV void dma_tilek(rsrc_t r, unsigned soff, unsigned slot, int i0, int l) {
  const unsigned rr = l >> 4, cp = l & 15;
  const unsigned sb = __builtin_amdgcn_readfirstlane(soff + i0 * 1024), lb = __builtin_amdgcn_readfirstlane(slot + i0 * 1024);
#pragma unroll
  for (int j = 0; j < 8; ++j) dma16(r, rr * 256 + ((cp ^ (4 * (j & 3) + rr)) * 16), sb + j * 1024, lb + j * 1024);
}
DEV v2u trread(LAS unsigned char *p) {
  return __builtin_bit_cast(v2u, __builtin_amdgcn_ds_read_tr16_b64_v4i16((LAS s4v *)p));
}
DEV void frags128(LAS unsigned char *slot, int m, int l, v4u (&f)[4]) {
  const unsigned g = l >> 4, q = (l & 15) >> 2, p = l & 3;
  const unsigned sw = q | ((g & 1) << 2);
  LAS unsigned char *a = slot + (8 * g + q) * 256 + ((m ^ sw) * 32) + p * 8;
#pragma unroll
  for (int s = 0; s < 4; ++s) {
    const v2u u0 = trread(a + s * 8192), u1 = trread(a + s * 8192 + 1024);
    f[s] = v4u{u0.x, u0.y, u1.x, u1.y};
  }
}
DEV void frags64(LAS unsigned char *slot, int m, int l, v4u (&f)[8]) {
  const unsigned g = l >> 4, q = (l & 15) >> 2, p = l & 3;
  const unsigned sw = ((q >> 1) & 1) | ((g & 1) << 1);
  LAS unsigned char *a = slot + (8 * g + q) * 128 + ((m ^ sw) * 32) + p * 8;
#pragma unroll
  for (int s = 0; s < 8; ++s) {
    const v2u u0 = trread(a + s * 4096), u1 = trread(a + s * 4096 + 512);
    f[s] = v4u{u0.x, u0.y, u1.x, u1.y};
  }
}
DEV v4u lds128(LAS unsigned char *p) { return *(LAS v4u *)p; }

DEV v4u ld128(rsrc_t r, unsigned v, unsigned s) { return __builtin_amdgcn_raw_buffer_load_b128(r, v, s, AUX_SC1); }
DEV v2u ld64(rsrc_t r, unsigned v, unsigned s) { return __builtin_amdgcn_raw_buffer_load_b64(r, v, s, AUX_SC1); }
DEV unsigned ld32(rsrc_t r, unsigned v, unsigned s) { return __builtin_amdgcn_raw_buffer_load_b32(r, v, s, AUX_SC1); }
DEV void st128(rsrc_t r, unsigned v, unsigned s, v4u d) { __builtin_amdgcn_raw_buffer_store_b128(d, r, v, s, AUX_SC1); }
DEV void st64(rsrc_t r, unsigned v, unsigned s, v2u d) { __builtin_amdgcn_raw_buffer_store_b64(d, r, v, s, AUX_SC1); }
DEV void st32(rsrc_t r, unsigned v, unsigned s, unsigned d) { __builtin_amdgcn_raw_buffer_store_b32(d, r, v, s, AUX_SC1); }
#define ALD64(d, r, v, s) asm volatile("buffer_load_dwordx2 %0, %1, %2, %3 offen sc1" : "=v"(d) : "v"(v), "s"(r), "s"(s) : "memory")
#define PLD64(d, r, v, s) asm volatile("buffer_load_dwordx2 %0, %1, %2, %3 offen" : "=v"(d) : "v"(v), "s"(r), "s"(s) : "memory")
DEV bool any_bad(bool bad) { return __builtin_amdgcn_ballot_w64(bad) != 0ull; }
#define LIKELY_NOT(x) __builtin_expect((x), 0)

struct Ctx {
  rsrc_t rs;
  LAS unsigned char *L;
  int c, tid, w, u, l;
  int K;
  int known;
  unsigned cap;
};
DEV unsigned half_of(const Ctx &C, int cls, int win) { return (unsigned)((cls ^ C.K ^ win) & 1) << 13; }
template <int LAYER> DEV unsigned half_key(const Ctx &C, int cls, int win, int bit) { return (unsigned)((cls ^ ((LAYER || C.known) ? C.K ^ win : bit)) & 1) << 13; }
DEV bool give_up(Ctx &C, unsigned &spin) {
  if (spin++ < C.cap) return false;
  C.cap = 0;
  return true;
}

DEV void half_sum2(float &a, float &b) {
  a += dppm<0xB1>(a); b += dppm<0xB1>(b); a += dppm<0x4E>(a); b += dppm<0x4E>(b);
  a += dppm<0x141>(a); b += dppm<0x141>(b); a += dppm<0x140>(a); b += dppm<0x140>(b);
  auto s = __builtin_amdgcn_permlane16_swap(__float_as_uint(a), __float_as_uint(a), false, false);
  auto t = __builtin_amdgcn_permlane16_swap(__float_as_uint(b), __float_as_uint(b), false, false);
  a = __uint_as_float(s[0]) + __uint_as_float(s[1]);
  b = __uint_as_float(t[0]) + __uint_as_float(t[1]);
}
DEV void row_stats(const v4u (&x)[4], float &mean, float &var) {
  float s1 = 0.f, s2 = 0.f, t1 = 0.f, t2 = 0.f;
#pragma unroll
  for (int i = 0; i < 4; ++i) {
    s1 = dot2(x[i].x, 0x3F803F80u, s1); s2 = dot2(x[i].x, x[i].x, s2);
    t1 = dot2(x[i].y, 0x3F803F80u, t1); t2 = dot2(x[i].y, x[i].y, t2);
    s1 = dot2(x[i].z, 0x3F803F80u, s1); s2 = dot2(x[i].z, x[i].z, s2);
    t1 = dot2(x[i].w, 0x3F803F80u, t1); t2 = dot2(x[i].w, x[i].w, t2);
  }
  s1 += t1; s2 += t2;
  half_sum2(s1, s2);
  mean = s1 * (1.0f / DM);
  var = vmax(s2 * (1.0f / DM) - mean * mean, 0.f);
}
template <int N> DEV bool sent_bad(const v4u (&x)[N]) {
  unsigned m = 0;
#pragma unroll
  for (int i = 0; i < N; ++i) m = umax(m, umax4(x[i]));
  return any_bad(m == SENT);
}
template <int K, int N> DEV bool quad_bad(const v4u (&v)[N]) {
  unsigned m = 0;
#pragma unroll
  for (int i = 0; i < N; ++i) m |= (v[i].x | v[i].y) | (v[i].z | v[i].w);
  return any_bad((m & (1u << K)) != 0);
}
template <int LAYER> DEV void gather_rows(Ctx &C, unsigned area, int kq, v4u (&x)[4]) {
  const unsigned voff = ((C.l >> 5) + 8 * ((C.l & 31) >> 3)) * 128 + (C.l & 7) * 16;
  const unsigned base = area + half_key<LAYER>(C, (C.c & 7) >> 2, 1, C.u) + 2 * C.u * 128;
  unsigned soff[4];
#pragma unroll
  for (int i = 0; i < 4; ++i) { const unsigned q = (kq + i) & 3; soff[i] = base + (q >> 1) * 16384 + (q & 1) * 4096; }
#pragma unroll
  for (int i = 0; i < 4; ++i) x[i] = ld128(C.rs, voff, soff[i]);
  if (LIKELY_NOT(sent_bad<4>(x))) {
    for (unsigned spin = 0; !give_up(C, spin);) {
      MEMFENCE();
#pragma unroll
      for (int i = 0; i < 4; ++i) x[i] = ld128(C.rs, voff, soff[i]);
      if (!sent_bad<4>(x)) break;
    }
  }
}
DEV LAS unsigned char *y_slot(Ctx &C) { return C.L + L_Y + (2 * C.u + (C.l >> 5)) * YS + (C.l & 31) * 16; }
DEV void stage_q_ln(Ctx &C, const v4u (&x)[4]) {
  float mean, var, f[8];
#pragma unroll
  for (int i = 0; i < 4; ++i) { f[2 * i] = lo16(x[0][i]); f[2 * i + 1] = hi16(x[0][i]); }
  row_stats(x, mean, var);
  const unsigned mv = pk(mean, var);
  const float mb = lo16(mv), sd = rbf(__builtin_amdgcn_sqrtf(rbf(hi16(mv) + EPS))), rs = __builtin_amdgcn_rcpf(sd);
  v4u y;
#pragma unroll
  for (int i = 0; i < 4; ++i) {
    const unsigned d = pk(f[2 * i] - mb, f[2 * i + 1] - mb);
    y[i] = pk(lo16(d) * rs, hi16(d) * rs);
  }
  *(LAS v4u *)y_slot(C) = y;
}
DEV f4 mm8(Ctx &C, const v4u (&wf)[8]) {
  LAS unsigned char *yb = C.L + L_Y + (C.l & 7) * YS + (C.l >> 4) * 16;
  v4u b[8];
#pragma unroll
  for (int s = 0; s < 8; ++s) b[s] = lds128(yb + s * 64);
  SCHED();
  f4 acc = {0.f, 0.f, 0.f, 0.f};
#pragma unroll
  for (int s = 0; s < 8; ++s) acc = mfma(wf[s], b[s], acc);
  return acc;
}
DEV f4 mm4x2(Ctx &C, unsigned base, const v4u (&wf)[8], v4u (&b)[4]) {
  LAS unsigned char *ab = C.L + base + (C.l & 7) * AS + (C.l >> 4) * 16;
#pragma unroll
  for (int s = 0; s < 4; ++s) b[s] = lds128(ab + s * 64);
  SCHED();
  f4 a0 = {0.f, 0.f, 0.f, 0.f}, a1 = {0.f, 0.f, 0.f, 0.f};
#pragma unroll
  for (int s = 0; s < 4; ++s) { a0 = mfma(wf[s], b[s], a0); a1 = mfma(wf[4 + s], b[s], a1); }
  const bool up = (C.l & 8) != 0;
  return f4{up ? a1.x : a0.x, up ? a1.y : a0.y, up ? a1.z : a0.z, up ? a1.w : a0.w};
}
DEV void frags128x2(LAS unsigned char *slot, int u, int l, v4u (&f)[8]) {
  v4u a[4], b[4];
  frags128(slot, 2 * u, l, a);
  frags128(slot, 2 * u + 1, l, b);
#pragma unroll
  for (int s = 0; s < 4; ++s) { f[s] = a[s]; f[4 + s] = b[s]; }
}
template <int LAYER>
DEV void stage_q_mm(Ctx &C, const v4u (&wf)[8], unsigned taglo, unsigned taghi) {
  const int l = C.l;
  const f4 acc = mm8(C, wf);
  if constexpr (LAYER == 0) {
    const bool up = (l & 8) != 0;
    const float a2 = dppm<0x128>(acc.z), a3 = dppm<0x128>(acc.w);
    const float v0 = up ? a2 : acc.x, v1 = up ? a3 : acc.y;
    const v4u o = {taglo, (__float_as_uint(v0) & 0xFFFFFF00u) | taghi, taglo, (__float_as_uint(v1) & 0xFFFFFF00u) | taghi};
    st128(C.rs, (l & 7) * 128 + ((l >> 3) & 1) * 64 + (l >> 4) * 16, WS_Q0 + C.c * Q0S + C.u * 1024, o);
  } else {
    if ((l & 15) < 8)
      st128(C.rs, half_of(C, (l >> 2) & 1, 1) + (l & 3) * 256 + (l >> 4) * 16, C_P2 + (C.c >> 3) * 16384 + (C.c & 7) * 1024 + C.u * 64, coded4<1>(acc));
  }
}
template <int LAYER, int LASTH> DEV float reduce_partials(Ctx &C, unsigned soff0) {
  const int l = C.l;
  unsigned soff[4];
#pragma unroll
  for (int i = 0; i < 4; ++i) soff[i] = soff0 + i * 8 * 16384 + half_key<LAYER>(C, (C.c & 7) >> 2, LAYER, i);
  const unsigned voff = (l >> 3) * 1024 + (l & 7) * 16, vlast = ((l >> 3) < LASTH ? (l >> 3) : (l >> 3) - 4) * 1024 + (l & 7) * 16;
  v4u v[4];
#pragma unroll
  for (int i = 0; i < 4; ++i) v[i] = ld128(C.rs, i == 3 ? vlast : voff, soff[i]);
  if (LIKELY_NOT(sent_bad<4>(v))) {
    for (unsigned spin = 0; !give_up(C, spin);) {
      MEMFENCE();
#pragma unroll
      for (int i = 0; i < 4; ++i) v[i] = ld128(C.rs, i == 3 ? vlast : voff, soff[i]);
      if (!sent_bad<4>(v)) break;
    }
  }
  if constexpr (LASTH < 8) {
    if ((l >> 3) >= LASTH) v[3] = v4u{0, 0, 0, 0};
  }
  unsigned one_lo = 0x00003F80u;
  asm("" : "+s"(one_lo));
  float a[8];
#pragma unroll
  for (int m = 0; m < 4; ++m) {
    a[2 * m] = lo16(v[0][m]); a[2 * m + 1] = hi16(v[0][m]);
#pragma unroll
    for (int i = 1; i < 4; ++i) { a[2 * m] = dot2(v[i][m], one_lo, a[2 * m]); a[2 * m + 1] = dot2(v[i][m], 0x3F800000u, a[2 * m + 1]); }
  }
  float r[4];
#pragma unroll
  for (int k = 0; k < 4; ++k) {
    auto t = __builtin_amdgcn_permlane32_swap(__float_as_uint(a[k]), __float_as_uint(a[k + 4]), false, false);
    r[k] = __uint_as_float(t[0]) + __uint_as_float(t[1]);
  }
  float q[2];
#pragma unroll
  for (int k = 0; k < 2; ++k) {
    auto t = __builtin_amdgcn_permlane16_swap(__float_as_uint(r[k]), __float_as_uint(r[k + 2]), false, false);
    q[k] = __uint_as_float(t[0]) + __uint_as_float(t[1]);
  }
  const bool up = (l & 8) != 0;
  return (up ? q[1] : q[0]) + dppm<0x128>(up ? q[0] : q[1]);
}
DEV unsigned line_pack(float v, int l) {
  const float o = dppm<0x128>(v);
  const bool up = (l & 8) != 0;
  return pk(up ? o : v, up ? v : o);
}

DEV void clear_share(Ctx &C) {
  st128(C.rs, C.l * 16, C.c * 8192 + C.w * 1024, v4u{SENT, SENT, SENT, SENT});
  st128(C.rs, C.l * 16, CH(1, 0) + C.c * 7168 + (C.w < 7 ? C.w : 6) * 1024, v4u{SENT, SENT, SENT, SENT});
}

DEV void early_verdict(Ctx &C) {
  const v4u n = *(LAS v4u *)(C.L + L_VC);
  const unsigned t = __builtin_amdgcn_readfirstlane(n.x + n.y + n.z + n.w);
  const int n0 = t & 0xFFFF, n1 = t >> 16;
  C.K = n1 > n0 ? 1 : 0;
  C.known = (n1 > n0 ? n1 - n0 : n0 - n1) >= 6;
}

struct FragsA { v4u wq[8], k[8], wo[8]; };

template <int LAYER>
DEV void layer_a(Ctx &C, const Params &p, FragsA &F, unsigned taglo, unsigned taghi, unsigned genlo, unsigned genhi) {
  const int c = C.c, u = C.u, l = C.l, tid = C.tid, g = l >> 4, hf = l >> 5, j = l & 31;
  LAS unsigned char *L = C.L;
  const int xc = c & 7, r = c >> 3;
  constexpr unsigned C_OB = LAYER ? C_OB1 : C_OB0, C_LSE = LAYER ? C_LSE1 : C_LSE0, C_X1 = LAYER ? C_X11 : C_X10, C_XP = LAYER ? C_XP1 : C_XP0;
  const int cls = xc >> 2;
  const int hh = l & 7, tau = (l >> 3) & 1;
  const int kr = xc + 8 * (r >> 3);
  if constexpr (LAYER == 0) {
    BAR();
    LAS unsigned char *kb = L + L_S2 + (32 * u + (l & 15)) * 256;
#pragma unroll
    for (int s = 0; s < 8; ++s) F.k[s] = lds128(kb + (s >> 2) * 4096 + (((4 * (s & 3) + g) ^ (l & 15)) * 16));
    BAR();
    const unsigned voff = (2 * hf + (j >> 4)) * Q0S + ((j >> 2) & 3) * 1024 + (j & 3) * 16;
    const unsigned soff = WS_Q0 + 2 * (8 * (r >> 3) + 2 * u) * Q0S + xc * 128;
    const unsigned vchk = l * Q0S, schk = WS_Q0 + 64 * u * Q0S + 4096;
    nap<D_Q0>();
    v4u v[8]; v2u chk;
#pragma unroll
    for (int i = 0; i < 8; ++i) v[i] = ld128(C.rs, voff, soff + (i >> 1) * 64 * Q0S + (i & 1) * 64);
    chk = ld64(C.rs, vchk, schk);
    auto bad = [&]() {
      bool b = chk.x != taglo || (chk.y & 0xFF) != taghi;
#pragma unroll
      for (int i = 0; i < 8; ++i) b |= v[i].x != taglo || v[i].z != taglo || (v[i].y & 0xFF) != taghi || (v[i].w & 0xFF) != taghi;
      return any_bad(b);
    };
    if (LIKELY_NOT(bad())) {
      for (unsigned spin = 0; !give_up(C, spin);) {
        MEMFENCE();
#pragma unroll
        for (int i = 0; i < 8; ++i) v[i] = ld128(C.rs, voff, soff + (i >> 1) * 64 * Q0S + (i & 1) * 64);
        chk = ld64(C.rs, vchk, schk);
        if (!bad()) break;
      }
    }
    float q[4] = {0.f, 0.f, 0.f, 0.f};
#pragma unroll
    for (int i = 0; i < 4; ++i) {
      q[0] += __uint_as_float(v[2 * i].y & 0xFFFFFF00u); q[1] += __uint_as_float(v[2 * i].w & 0xFFFFFF00u);
      q[2] += __uint_as_float(v[2 * i + 1].y & 0xFFFFFF00u); q[3] += __uint_as_float(v[2 * i + 1].w & 0xFFFFFF00u);
    }
    *(LAS v2u *)(L + L_Q + (2 * u + hf) * AS + j * 8) = v2u{pk(q[0], q[1]), pk(q[2], q[3])};
    const unsigned n0 = __builtin_popcountll(__builtin_amdgcn_ballot_w64(((chk.y >> 8) & 3) == 1));
    const unsigned n1 = __builtin_popcountll(__builtin_amdgcn_ballot_w64(((chk.y >> 8) & 3) == 2));
    if (l == 0) *(LAS unsigned *)(L + L_VC + u * 4) = n0 | (n1 << 16);
  } else {
    const unsigned voff = (2 * hf + (j >> 4)) * 1024 + (j & 15) * 16;
    const unsigned soff = C_P2 + (2 * (r >> 3) + (u >> 1)) * 16384 + half_of(C, cls, 1) + (u & 1) * 4096 + (xc & 3) * 256;
    nap<D_Q1>();
    v4u v[4];
#pragma unroll
    for (int i = 0; i < 4; ++i) v[i] = ld128(C.rs, voff, soff + i * 8 * 16384);
    if (LIKELY_NOT((quad_bad<1, 4>(v)))) {
      for (unsigned spin = 0; !give_up(C, spin);) {
        MEMFENCE();
#pragma unroll
        for (int i = 0; i < 4; ++i) v[i] = ld128(C.rs, voff, soff + i * 8 * 16384);
        if (!quad_bad<1, 4>(v)) break;
      }
    }
    const f4 q = (__builtin_bit_cast(f4, v[0]) + __builtin_bit_cast(f4, v[1])) + (__builtin_bit_cast(f4, v[2]) + __builtin_bit_cast(f4, v[3]));
    *(LAS v2u *)(L + L_Q + (2 * u + hf) * AS + j * 8) = v2u{pk(q.x, q.y), pk(q.z, q.w)};
  }
  BAR();
  if constexpr (LAYER == 0) early_verdict(C);
  if constexpr (LAYER == 0) {
    if (c == 0 && tid == 0) st64(C.rs, 0, WS_GEN, v2u{genlo, genhi});
  }
  v4u b[4];
  const f4 qk = mm4x2(C, L_Q, F.k, b);
  const unsigned a01 = pk(qk.x, qk.y), a23 = pk(qk.z, qk.w);
  const unsigned s01 = pk(lo16(a01) * QK_SCALE, hi16(a01) * QK_SCALE), s23 = pk(lo16(a23) * QK_SCALE, hi16(a23) * QK_SCALE);
  const float sc0 = lo16(s01), sc1 = hi16(s01), sc2 = lo16(s23), sc3 = hi16(s23);
  float wm = vmax(vmax3(sc0, sc1, sc2), sc3);
  wm = xrow_max(vmax(wm, dppm<0x128>(wm)));
  if (l < 8) *(LAS float *)(L + L_M + l * 16 + u * 4) = wm;
  BAR();
  {
    const f4 m = *(LAS f4 *)(L + L_M + hh * 16);
    const float Ml = vmax(vmax3(m.x, m.y, m.z), m.w) * LOG2E;
    const float e0 = __builtin_amdgcn_exp2f(fmaf(sc0, LOG2E, -Ml)), e1 = __builtin_amdgcn_exp2f(fmaf(sc1, LOG2E, -Ml));
    const float e2 = __builtin_amdgcn_exp2f(fmaf(sc2, LOG2E, -Ml)), e3 = __builtin_amdgcn_exp2f(fmaf(sc3, LOG2E, -Ml));
    *(LAS v2u *)(L + L_P + hh * AS + (32 * u + 16 * tau + 4 * g) * 2) = v2u{pk(e0, e1), pk(e2, e3)};
  }
  BAR();
  {
    const unsigned base = C_OB + 8 * (kr >> 3) * 16384 + (kr & 3) * 256;
    unsigned soff[8];
#pragma unroll
    for (int s = 0; s < 8; ++s) soff[s] = base + s * 16384 + half_key<LAYER>(C, cls, 0, s) + ((u + s) & 3) * 2048;
    const unsigned soffl = C_LSE + (kr >> 3) * 16384 + half_key<LAYER>(C, cls, 1, 0) + 2 * u * 128 + (kr & 3) * 4;
    const unsigned voff = hf * 1024 + j * 8, voffl = (hf + 8 * (l & 7)) * 128;
    v2u v[8]; unsigned lse;
    nap<LAYER ? D_O1 : D_O0>();
    lse = ld32(C.rs, voffl, soffl);
#pragma unroll
    for (int s = 0; s < 8; ++s) v[s] = ld64(C.rs, voff, soff[s]);
    auto bad = [&]() {
      unsigned m = lse;
#pragma unroll
      for (int s = 0; s < 8; ++s) m = umax(m, umax(v[s].x, v[s].y));
      return any_bad(m == SENT);
    };
    if (LIKELY_NOT(bad())) {
      for (unsigned spin = 0; !give_up(C, spin);) {
        MEMFENCE();
        lse = ld32(C.rs, voffl, soffl);
#pragma unroll
        for (int s = 0; s < 8; ++s) v[s] = ld64(C.rs, voff, soff[s]);
        if (!bad()) break;
      }
    }
    const float ls = __uint_as_float(lse);
    float mx = vmax(ls, dppm<0xB1>(ls)); mx = vmax(mx, dppm<0x4E>(mx)); mx = vmax(mx, dppm<0x141>(mx));
    const float e = __builtin_amdgcn_exp2f(ls - mx);
    float sm = e + dppm<0xB1>(e); sm += dppm<0x4E>(sm); sm += dppm<0x141>(sm);
    const float wt = e * __builtin_amdgcn_rcpf(sm);
    f2 o0 = {0.f, 0.f}, o1 = {0.f, 0.f};
#define COMBINE(s) { const float ws = dppm<0x150 + s>(wt); const f2 w2 = {ws, ws}; \
      o0 = __builtin_elementwise_fma(w2, f2{lo16(v[s].x), hi16(v[s].x)}, o0); o1 = __builtin_elementwise_fma(w2, f2{lo16(v[s].y), hi16(v[s].y)}, o1); }
    COMBINE(0) COMBINE(1) COMBINE(2) COMBINE(3) COMBINE(4) COMBINE(5) COMBINE(6) COMBINE(7)
#undef COMBINE
    *(LAS v2u *)(L + L_A + (2 * u + hf) * AS + j * 8) = v2u{pk(o0.x, o0.y), pk(o1.x, o1.y)};
  }
  BAR();
  if constexpr (LAYER == 0) frags128x2(L + L_S2, u, l, F.wo);
  {
    const f4 acc = mm4x2(C, L_A, F.wo, b);
    st64(C.rs, half_key<LAYER>(C, hh >> 2, LAYER, c >> 6) + (hh & 3) * 256 + tau * 32 + g * 8, C_XP + (c >> 3) * 16384 + (c & 7) * 1024 + u * 64,
         v2u{nosent(pk(acc.x, acc.y)), nosent(pk(acc.z, acc.w))});
  }
  if (c < 128 && u == 0) {
    const int t3 = c & 7, cc = c >> 3;
    const unsigned soff = C_XP + (cc >> 1) * 16384 + (t3 & 3) * 256 + (cc & 1) * 128;
    const float xr = *(LAS float *)(L + L_XR + l * 4);
    nap<LAYER ? D_XP1 : D_XP0>();
    const float a = reduce_partials<LAYER, 8>(C, soff);
    const float x1 = rbf(xr + rbf(a));
    st32(C.rs, (((l >> 3) & 1) << 13) + (l & 7) * 16 + g * 4, C_X1 + (c >> 6) * 16384 + (c & 63) * 128, nosent(line_pack(x1, l)));
    *(LAS float *)(L + L_XR + l * 4) = x1;
  }
  BAR();
  if constexpr (LAYER == 0) {
    if (c < 32 && u >= 2) {
      const unsigned so = C_PROBE + ((u & 1) << 13) + c * 128;
      st32(C.rs, 0, so, (unsigned)c);
      VMWAIT(0);
      unsigned long long t0, t1;
      asm volatile("s_memtime %0\n\ts_waitcnt lgkmcnt(0)" : "=s"(t0) :: "memory");
      unsigned pv = ld32(C.rs, 0, so);
      asm volatile("s_waitcnt vmcnt(0)" : "+v"(pv) :: "memory");
      asm volatile("s_memtime %0\n\ts_waitcnt lgkmcnt(0)" : "=s"(t1) :: "memory");
      if (l == 0) *(LAS unsigned *)(L + L_PR + (u & 1) * 4) = (unsigned)(t1 - t0);
    }
  }
  BAR();
  if constexpr (LAYER == 0) {
    if (c < 32 && u == 2) {
      const v2u d = *(LAS v2u *)(L + L_PR);
      const unsigned k = (d.y < d.x ? 1u : 0u) ^ (xc >> 2) ^ 1u;
      if (l == 0) st64(C.rs, 0, WS_VOTE + c * 128, v2u{taglo, (k << 8) | taghi});
    }
  }
  if constexpr (LAYER == 0) {
    nap<D_X2>();
    v4u x[4];
    v2u vt = {0, 0};
    if (u == 0) vt = ld64(C.rs, (l & 31) * 128, WS_VOTE);
    gather_rows<0>(C, C_X2, c >> 6, x);
    if (u == 0) {
      for (unsigned spin = 0; any_bad(vt.x != taglo || (vt.y & 0xFF) != taghi) && !give_up(C, spin);) {
        MEMFENCE();
        vt = ld64(C.rs, (l & 31) * 128, WS_VOTE);
      }
      const unsigned long long ones = __builtin_amdgcn_ballot_w64(((vt.y >> 8) & 1) != 0);
      if (l == 0) *(LAS unsigned *)(L + L_PR + 8) = __builtin_popcountll(ones) > 32 ? 1u : 0u;
    }
    stage_q_ln(C, x);
    BAR();
    C.K = __builtin_amdgcn_readfirstlane(*(LAS unsigned *)(L + L_PR + 8));
    stage_q_mm<1>(C, F.wq, 0, 0);
  }
}

DEV void group_a(Ctx &C, const Params &p, unsigned line, unsigned long long pc) {
  const int c = C.c, u = C.u, l = C.l;
  v4u x[4]; v2u gen; unsigned xr, warm;
  const rsrc_t rx = mkrsrc(p.x, 8 * DM * 2);
  const int kq = c >> 6;
#pragma unroll
  for (int i = 0; i < 4; ++i)
    asm volatile("buffer_load_dwordx4 %0, %1, %2, %3 offen" : "=v"(x[i]) : "v"((l >> 5) * 2048 + (l & 31) * 16), "s"(rx), "s"(u * 4096 + ((kq + i) & 3) * 512) : "memory");
  ALD64(gen, C.rs, 0, WS_GEN);
  dma_tile64<8>(mkrsrc(p.wq, DM * 4096 * 2), (256 * kq * 4096 + 64 * (c & 63)) * 2, 8192, L_S0, 8 * u, l);
  const unsigned rcol = 64 * ((c >> 3) & 15) + 8 * (l & 7) + (l >> 3);
  asm volatile("buffer_load_ushort %0, %1, %2, %3 offen" : "=v"(xr) : "v"(rcol * 2), "s"(rx), "s"((c & 7) * DM * 2) : "memory");
  asm volatile("global_load_dword %0, %1, %2" : "=v"(warm) : "v"(line * 128), "s"(pc) : "memory");
  clear_share(C);
  asm volatile("s_waitcnt vmcnt(12)" : "+v"(gen), "+v"(x[0]), "+v"(x[1]), "+v"(x[2]), "+v"(x[3]) :: "memory");
  const unsigned glo = __builtin_amdgcn_readfirstlane(gen.x), ghi = __builtin_amdgcn_readfirstlane(gen.y);
  const unsigned genlo = glo + 1, genhi = ghi + (genlo == 0 ? 1 : 0);
  const unsigned taglo = genlo, taghi = genhi & 0xFF;
  if (u == 0 && l == 0) *(LAS v2u *)(C.L + L_PR) = v2u{taglo, taghi};
  stage_q_ln(C, x);
  asm volatile("s_waitcnt vmcnt(0)" : "+v"(xr), "+v"(warm) :: "memory");
  if (u == 0) *(LAS float *)(C.L + L_XR + l * 4) = lo16(xr);
  BAR();
  FragsA F;
  frags64(C.L + L_S0, u, l, F.wq);
  stage_q_mm<0>(C, F.wq, taglo, taghi);
  BAR();
  layer_a<0>(C, p, F, taglo, taghi, genlo, genhi);
  layer_a<1>(C, p, F, taglo, taghi, genlo, genhi);
}

struct FragsB { v4u v[8], w1[8], w2[8]; };

template <int LAYER>
DEV void layer_b(Ctx &C, const Params &p, FragsB &F, v2u b1v, float b2f) {
  const int c = C.c, u = C.u, l = C.l, g = l >> 4, hf = l >> 5, j = l & 31;
  LAS unsigned char *L = C.L;
  const int xc = c & 7, r = c >> 3;
  constexpr unsigned C_OB = LAYER ? C_OB1 : C_OB0, C_LSE = LAYER ? C_LSE1 : C_LSE0, C_X1 = LAYER ? C_X11 : C_X10, C_X2P = LAYER ? C_X2P1 : C_X2P0;
  constexpr int K_HP = 2 * LAYER;
  const int cls = xc >> 2;
  const int hh = l & 7, tau = (l >> 3) & 1;
  const int kr = xc + 8 * (r >> 3);
  const bool act4 = c < 224, act5 = kr < 28;
  if constexpr (LAYER == 0) {
    const unsigned kvoff = ((xc * 4 + (r >> 3)) * SK + (SK - WIN) + 128 * (r & 7)) * 256;
    dma_tile128<8>(mkrsrc(p.vc, 8 * 4 * SK * 256), kvoff, 256, L_S1, 8 * u, l);
    VMWAIT(8);
    BAR();
    BAR();
    dma_tile128<8>(mkrsrc(p.wo, 4096 * DM * 2), (128 * kr * DM + 128 * (r & 7)) * 2, DM * 2, L_S2, 8 * u, l);
  }
  BAR();
  if constexpr (LAYER == 0) early_verdict(C);
  BAR();
  const f4 m = *(LAS f4 *)(L + L_M + hh * 16);
  const float Ml = vmax(vmax3(m.x, m.y, m.z), m.w) * LOG2E;
  if constexpr (LAYER == 0) {
    VMWAIT(8);
  }
  BAR();
  v4u b[4];
  {
    if constexpr (LAYER == 0) frags128x2(L + L_S1, u, l, F.v);
    const f4 o = mm4x2(C, L_P, F.v, b);
    float ls = 0.f, lt = 0.f;
#pragma unroll
    for (int s = 0; s < 4; ++s) {
      ls = dot2(b[s].x, 0x3F803F80u, ls); lt = dot2(b[s].y, 0x3F803F80u, lt);
      ls = dot2(b[s].z, 0x3F803F80u, ls); lt = dot2(b[s].w, 0x3F803F80u, lt);
    }
    const float Ls = xrow_sum(ls + lt), inv = __builtin_amdgcn_rcpf(Ls);
    st64(C.rs, half_key<LAYER>(C, hh >> 2, 0, r) + (hh & 3) * 256 + tau * 32 + g * 8, C_OB + r * 16384 + ((xc + 2 * (r & 7)) & 7) * 1024 + u * 64,
         v2u{nosent(pk(o.x * inv, o.y * inv)), nosent(pk(o.z * inv, o.w * inv))});
    if (u == 0 && l < 8)
      st32(C.rs, half_key<LAYER>(C, l >> 2, 1, 0) + (l & 3) * 4, C_LSE + (c >> 6) * 16384 + (c & 63) * 128, nosentf(Ml + __builtin_amdgcn_logf(Ls)));
  }
  if constexpr (LAYER == 0) {
    if (act4) {
      dma_tile64<8>(mkrsrc(p.w1, DM * DFF * 2), (256 * (c / 56) * DFF + 64 * (c % 56)) * 2, DFF * 2, L_S3, 8 * u, l);
      VMWAIT(9);
    } else {
      VMWAIT(1);
    }
  }
  BAR();
  if constexpr (LAYER == 0) {
    nap<D_W2>();
    if (act5) dma_tile128<8>(mkrsrc(p.w2, DFF * DM * 2), (128 * kr * DM + 128 * (r & 7)) * 2, DM * 2, L_S0, 8 * u, l);
  }
  {
    nap<LAYER ? D_X11 : D_X10>();
    v4u x[4];
    gather_rows<LAYER>(C, C_X1, act4 ? c / 56 : 0, x);
    if constexpr (LAYER == 0) VMWAIT(0);
    float mean, var;
    row_stats(x, mean, var);
    const float r1 = __builtin_amdgcn_rsqf(var + EPS), v2 = var * r1 * r1, rr = r1 * __builtin_amdgcn_rsqf(v2 + EPS), mr = -mean * rr;
    v4u y;
#pragma unroll
    for (int i = 0; i < 4; ++i) y[i] = pk(fmaf(lo16(x[0][i]), rr, mr), fmaf(hi16(x[0][i]), rr, mr));
    *(LAS v4u *)y_slot(C) = y;
  }
  BAR();
  if (act4) {
    if constexpr (LAYER == 0) frags64(L + L_S3, u, l, F.w1);
    const f4 acc = mm8(C, F.w1);
    const int ct = c % 56, kc = ct >> 1;
    if ((l & 15) < 8)
      st128(C.rs, (l & 15) * 64 + g * 16,
            C_P2 + (2 * ((kc & 3) + 4 * (kc >> 3)) + (ct & 1)) * 16384 + half_key<LAYER>(C, (kc & 7) >> 2, 1, c / 56) + (c / 56) * 2048 + u * 512, coded4<K_HP>(acc));
  }
  if (act5) {
    const unsigned sb = C_P2 + (2 * ((kr & 3) + 4 * (kr >> 3)) + (u >> 1)) * 16384 + (u & 1) * 1024, voff = l * 16;
    unsigned soff[4];
#pragma unroll
    for (int i = 0; i < 4; ++i) soff[i] = sb + i * 2048 + half_key<LAYER>(C, cls, 1, i);
    nap<LAYER ? D_HP1 : D_HP0>();
    v4u v[4];
#pragma unroll
    for (int i = 0; i < 4; ++i) v[i] = ld128(C.rs, voff, soff[i]);
    if (LIKELY_NOT((quad_bad<K_HP, 4>(v)))) {
      for (unsigned spin = 0; !give_up(C, spin);) {
        MEMFENCE();
#pragma unroll
        for (int i = 0; i < 4; ++i) v[i] = ld128(C.rs, voff, soff[i]);
        if (!quad_bad<K_HP, 4>(v)) break;
      }
    }
    const f4 pre = (__builtin_bit_cast(f4, v[0]) + __builtin_bit_cast(f4, v[1])) + (__builtin_bit_cast(f4, v[2]) + __builtin_bit_cast(f4, v[3]));
    const unsigned p01 = pk(pre.x, pre.y), p23 = pk(pre.z, pre.w);
    const unsigned h01 = pk(lo16(p01) + lo16(b1v.x), hi16(p01) + hi16(b1v.x)), h23 = pk(lo16(p23) + lo16(b1v.y), hi16(p23) + hi16(b1v.y));
    const float h[4] = {lo16(h01), hi16(h01), lo16(h23), hi16(h23)};
    constexpr float GC = -2.0f * 0.7978845608028654f * LOG2E;
    float gl[4];
#pragma unroll
    for (int i = 0; i < 4; ++i)
      gl[i] = h[i] * __builtin_amdgcn_rcpf(1.0f + __builtin_amdgcn_exp2f(h[i] * fmaf(GC * 0.044715f * h[i], h[i], GC)));
    *(LAS v2u *)(L + L_A + (j >> 2) * AS + (16 * (2 * u + hf) + 4 * (j & 3)) * 2) = v2u{pk(gl[0], gl[1]), pk(gl[2], gl[3])};
  }
  BAR();
  if (act5) {
    if constexpr (LAYER == 0) frags128x2(L + L_S0, u, l, F.w2);
    const f4 acc = mm4x2(C, L_A, F.w2, b);
    st64(C.rs, half_key<LAYER>(C, hh >> 2, LAYER, c >> 6) + (hh & 3) * 256 + tau * 32 + g * 8, C_X2P + (c >> 3) * 16384 + (c & 7) * 1024 + u * 64,
         v2u{nosent(pk(acc.x, acc.y)), nosent(pk(acc.z, acc.w))});
  }
  if (c < 128 && u == 0) {
    const int t3 = c & 7, cc = c >> 3;
    const unsigned soff = C_X2P + (cc >> 1) * 16384 + (t3 & 3) * 256 + (cc & 1) * 128;
    const float xr = *(LAS float *)(L + L_XR + l * 4);
    nap<LAYER ? D_X2P1 : D_X2P0>();
    const float a = reduce_partials<LAYER, 4>(C, soff);
    const float x2 = rbf(xr + rbf(rbf(a) + b2f));
    const unsigned d = line_pack(x2, l);
    if constexpr (LAYER == 0) {
      st32(C.rs, (((l >> 3) & 1) << 13) + (l & 7) * 16 + g * 4, C_X2 + (c >> 6) * 16384 + (c & 63) * 128, nosent(d));
      *(LAS float *)(L + L_XR + l * 4) = x2;
    } else {
      if ((l & 8) == 0) *(GAS unsigned *)(p.out + t3 * DM + 64 * cc + (l & 7) * 8 + g * 2) = d;
    }
  }
  if constexpr (LAYER == 0) {
    BAR();
    C.K = __builtin_amdgcn_readfirstlane(*(LAS unsigned *)(L + L_PR + 8));
  }
}

DEV void group_b(Ctx &C, const Params &p, unsigned line, unsigned long long pc, unsigned vote) {
  const int c = C.c, u = C.u, l = C.l, xc = c & 7, r = c >> 3;
  const int kr = xc + 8 * (r >> 3), kra = kr < 28 ? kr : 0;
  const unsigned rcol = 64 * ((c >> 3) & 15) + 8 * (l & 7) + (l >> 3);
  v2u b1v; unsigned b2u, warm;
  PLD64(b1v, mkrsrc(p.b1, DFF * 2), ((l >> 5) * 16 + (l & 3) * 4) * 2, (128 * kra + 32 * u) * 2);
  asm volatile("buffer_load_ushort %0, %1, %2, %3 offen" : "=v"(b2u) : "v"(rcol * 2), "s"(mkrsrc(p.b2, DM * 2)), "s"(0) : "memory");
  asm volatile("global_load_dword %0, %1, %2" : "=v"(warm) : "v"(line * 128), "s"(pc) : "memory");
  clear_share(C);
  __builtin_amdgcn_s_sleep(2);
  const unsigned kvoff = ((xc * 4 + (r >> 3)) * SK + (SK - WIN) + 128 * (r & 7)) * 256;
  dma_tilek(mkrsrc(p.kc, 8 * 4 * SK * 256), kvoff, L_S2, 8 * u, l);
  asm volatile("s_waitcnt vmcnt(8)" : "+v"(b1v), "+v"(b2u), "+v"(warm) :: "memory");
  BAR();
  if (u == 0) {
    const v2u tg = *(LAS v2u *)(C.L + L_PR);
    if (l == 0) st64(C.rs, 0, WS_Q0 + c * Q0S + 4096, v2u{tg.x, (vote << 8) | tg.y});
  }
  BAR();
  FragsB F;
  const float b2f = lo16(b2u);
  layer_b<0>(C, p, F, b1v, b2f);
  layer_b<1>(C, p, F, b1v, b2f);
}

__global__ void __launch_bounds__(NT, 2) fused_decode_kernel(Params p) {
  __shared__ __attribute__((aligned(16))) unsigned char smem[LDS_BYTES];
  asm volatile("" : "+s"(p.x), "+s"(p.kc), "+s"(p.vc), "+s"(p.wq), "+s"(p.wo), "+s"(p.w1), "+s"(p.b1), "+s"(p.w2), "+s"(p.b2), "+s"(p.out), "+s"(p.ws));
  Ctx C;
  C.L = (LAS unsigned char *)smem;
  C.c = blockIdx.x; C.tid = threadIdx.x; C.l = threadIdx.x & 63;
  C.w = __builtin_amdgcn_readfirstlane(threadIdx.x >> 6);
  C.u = C.w & 3;
  C.rs = mkrsrc((void *)(((uintptr_t)p.ws + (4u << 20) - 1) & ~(uintptr_t)((4u << 20) - 1)), WS_BYTES);
  C.K = 0;
  C.known = 0;
  C.cap = SPIN_CAP;
  unsigned long long pc;
  asm volatile("s_getpc_b64 %0" : "=s"(pc));
  unsigned line = (C.c >> 3) * 8 + C.w;
  line = line < CODE_LINES ? line : CODE_LINES - 1;
  unsigned vote = 0;
  if (C.w == 4) {
    const unsigned first = (C.c >> 3) & 1, a = C_PROBE + (C.c >> 6) * 16384 + (C.c & 63) * 128;
    unsigned v0 = 0, v1 = 0;
    unsigned long long t1, t2;
    if (C.l == 0)
      asm volatile("buffer_atomic_add %0, %2, %4, 0 offen sc0 sc1\n\tbuffer_atomic_add %1, %3, %4, 0 offen sc0 sc1"
                   : "+v"(v0), "+v"(v1) : "v"(a + first * 8192), "v"(a + (first ^ 1) * 8192), "s"(C.rs) : "memory");
    asm volatile("s_waitcnt vmcnt(1)\n\ts_memtime %0\n\ts_waitcnt lgkmcnt(0)" : "=s"(t1) :: "memory");
    asm volatile("s_waitcnt vmcnt(0)\n\ts_memtime %0\n\ts_waitcnt lgkmcnt(0)" : "=s"(t2), "+v"(v0), "+v"(v1) :: "memory");
    if ((unsigned)(t2 - t1) > 150) vote = 1 + (first ^ ((C.c & 7) >> 2) ^ 1);
  }
  if (__builtin_expect(C.w < 4, 1)) group_a(C, p, line, pc);
  else group_b(C, p, line, pc, vote);
}

}

extern "C" void kernel_launch(void *const *d_in, const int *in_sizes, int n_in, void *d_out, int out_size, void *d_ws,
                              size_t ws_size, hipStream_t stream) {
  Params p;
  p.x = (const u16 *)d_in[0]; p.kc = (const u16 *)d_in[1]; p.vc = (const u16 *)d_in[2];
  p.wq = (const u16 *)d_in[3]; p.wo = (const u16 *)d_in[4];
  p.w1 = (const u16 *)d_in[6]; p.b1 = (const u16 *)d_in[7]; p.w2 = (const u16 *)d_in[8]; p.b2 = (const u16 *)d_in[9];
  p.out = (u16 *)d_out; p.ws = (unsigned char *)d_ws;
  hipLaunchKernelGGL(fused_decode_kernel, dim3(NCU), dim3(NT), 0, stream, p);
}
```
